```python
import torch

def _softmax(x, axis=-1):
    return torch.softmax(x, dim=axis)
def _gelu(x):
    return torch.nn.functional.gelu(x, approximate="tanh")
def _silu(x):
    return torch.nn.functional.silu(x)
def _sigmoid(x):
    return torch.sigmoid(x)
def _layernorm(x, axis=-1, eps=1e-5):
    m = x.mean(dim=axis, keepdim=True)
    v = x.var(dim=axis, keepdim=True, unbiased=False)
    return (x - m) / torch.sqrt(v + eps)
def _scatter_add(tgt, idx, src, axis=0):
    return tgt.clone().index_add_(axis, idx, src)
def _segment_sum(src, seg, n):
    out = torch.zeros((n,) + src.shape[1:], dtype=src.dtype)
    return out.index_add_(0, seg, src)
def _pad(x, pw):
    flat = tuple(v for pair in reversed(pw) for v in pair)
    return torch.nn.functional.pad(x, flat)

kernel_name = 'gated_mix__mha_block_cached__mixer_channel_mlp__decode'

def setup_inputs():

    _w = (lambda a: (lambda t: t.to(torch.bfloat16) if t.is_floating_point() else t)(torch.as_tensor(a)))
    def _A_setup():
        B, Sq, D = 8, 1, 576
        H, Sk, Dh = 32, 4096, 128
        return {
            "x": _w((lambda *s: torch.randn(*s, dtype=torch.bfloat16) * 0.05)(B, Sq, D)),
            "k_cache": _w((lambda *s: torch.randn(*s, dtype=torch.bfloat16) * 6.4)(B, H, Sk, Dh)),
            "v_cache": _w((lambda *s: torch.randn(*s, dtype=torch.bfloat16) * 0.4)(B, H, Sk, Dh)),
            "wq": _w((lambda *s: torch.randn(*s, dtype=torch.bfloat16) * 6.4)(D, H * Dh) * (D ** -0.5)),
            "wo": _w((lambda *s: torch.randn(*s, dtype=torch.bfloat16) * 0.4)(H * Dh, D) * ((H * Dh) ** -0.5)),
        }
    _A_in = _A_setup()

    def _B_setup():
        B, S, D, F = 8, 1, 576, 4608
        return {
            "x": _w((lambda *s: torch.randn(*s, dtype=torch.bfloat16) * 0.05)(B, S, D)),
            "w1": _w((lambda *s: torch.randn(*s, dtype=torch.bfloat16) * 0.8)(D, F) * (D ** -0.5)),
            "b1": _w((lambda *s: torch.randn(*s, dtype=torch.bfloat16) * 0.8)(F)),
            "w2": _w((lambda *s: torch.randn(*s, dtype=torch.bfloat16) * 0.05)(F, D) * (F ** -0.5)),
            "b2": _w((lambda *s: torch.randn(*s, dtype=torch.bfloat16) * 0.05)(D)),
        }
    _B_in = _B_setup()

    return {
        "A_x": _A_in["x"],
        "A_k_cache": _A_in["k_cache"],
        "A_v_cache": _A_in["v_cache"],
        "A_wq": _A_in["wq"],
        "A_wo": _A_in["wo"],
        "B_x": _B_in["x"],
        "B_w1": _B_in["w1"],
        "B_b1": _B_in["b1"],
        "B_w2": _B_in["w2"],
        "B_b2": _B_in["b2"],
        "gate_w": _w((lambda *s: torch.randn(*s, dtype=torch.bfloat16) * 0.05)(576) * 16.0),
    }

def reference(A_x, A_k_cache, A_v_cache, A_wq, A_wo, B_x, B_w1, B_b1, B_w2, B_b2, gate_w):
    def _A_ref(x, k_cache, v_cache, wq, wo):
        B, Sq, D = x.shape
        H, Dh = 32, 128
        q = (lambda x, *d: x.permute(*d))((x @ wq).reshape(B, Sq, H, Dh), 0, 2, 1, 3)
        scores = torch.einsum("bhqd,bhkd->bhqk", q, k_cache) * (Dh ** -0.5)
        p = _softmax(scores, axis=-1)
        o = torch.einsum("bhqk,bhkd->bhqd", p, v_cache)
        return (lambda x, *d: x.permute(*d))(o, 0, 2, 1, 3).reshape(B, Sq, H * Dh) @ wo

    def _B_ref(x, w1, b1, w2, b2):
        y = _layernorm(x, axis=-1)
        h = _gelu(torch.einsum("bsd,df->bsf", y, w1) + b1)
        return torch.einsum("bsf,fd->bsd", h, w2) + b2

    x = A_x
    g = _sigmoid((x * gate_w).sum(-1, keepdims=True))
    a = _A_ref(x, A_k_cache, A_v_cache, A_wq, A_wo)
    b = _B_ref(x, B_w1, B_b1, B_w2, B_b2)
    return g * a + (1 - g) * b
```

```cpp
#include <hip/hip_runtime.h>
#include <hip/hip_bf16.h>
#include <stdint.h>

namespace gm {

constexpr int NB = 8, NH = 32, SK = 4096, DH = 128, DM = 576, FF = 4608;
constexpr int FS = FF / NH;
constexpr int NWS = 8, NWA = 4, NTHR = (NWS + NWA) * 64;
constexpr int TK = 16;
constexpr int NT = SK / TK / NWS;
#define GM_DEPTH 2
constexpr int DEPTH = GM_DEPTH;
#define GM_WNT 0

constexpr int L_WO = 0;
constexpr int L_PART = 128 * 1152;
constexpr int L_MLP = L_PART + 4096;
constexpr int L_Y = L_MLP + 3 * 2304;
constexpr int L_HID = L_Y + 3 * 1152;
constexpr int L_ML = L_HID + 3 * 192;
constexpr int L_OBF = L_ML + 64;
constexpr int L_MISC = L_OBF + 256;
constexpr int L_BYTES = L_MISC + 16;
static_assert(L_BYTES <= 163840, "LDS");

constexpr size_t W_EPOCH = 0;
constexpr size_t W_INBOX = 4096;
constexpr size_t W_BYTES = W_INBOX + (size_t)NB * 36 * 32 * 16 * 8;

#define GAS __attribute__((address_space(1)))
#define LAS __attribute__((address_space(3)))
typedef unsigned v4u __attribute__((ext_vector_type(4)));
typedef unsigned v2u __attribute__((ext_vector_type(2)));
typedef float f32x4 __attribute__((ext_vector_type(4)));
typedef short s16x4 __attribute__((ext_vector_type(4)));
typedef __bf16 bf16x2 __attribute__((ext_vector_type(2)));
typedef __bf16 bf16x8 __attribute__((ext_vector_type(8)));
typedef unsigned long long u64;
typedef unsigned short u16;

struct Args {
  const u16* x; const u16* k; const u16* v; const u16* wq; const u16* wo;
  const u16* w1; const u16* b1; const u16* w2; const u16* b2; const u16* gw;
  u16* out; unsigned char* ws;
};

__device__ __forceinline__ v4u ld_nt(const GAS v4u* p) { return __builtin_nontemporal_load(p); }
__device__ __forceinline__ v4u ld_w(const GAS v4u* p) {
  return *p;
}
__device__ __forceinline__ float bf2f(unsigned h) { return __builtin_bit_cast(float, h << 16); }
__device__ __forceinline__ float bflo(unsigned u) { return __builtin_bit_cast(float, u << 16); }
__device__ __forceinline__ float bfhi(unsigned u) { return __builtin_bit_cast(float, u & 0xffff0000u); }
__device__ __forceinline__ unsigned pk_bf16(float lo, float hi) {
  bf16x2 p = {(__bf16)lo, (__bf16)hi};
  return __builtin_bit_cast(unsigned, p);
}
__device__ __forceinline__ float rbf(float f) { return (float)(__bf16)f; }
__device__ __forceinline__ float dot2(unsigned a, unsigned b, float c) {
  return __builtin_amdgcn_fdot2_f32_bf16(__builtin_bit_cast(bf16x2, a), __builtin_bit_cast(bf16x2, b), c, false);
}
__device__ __forceinline__ f32x4 mfma(v4u a, v4u b, f32x4 c) {
  return __builtin_amdgcn_mfma_f32_16x16x32_bf16(__builtin_bit_cast(bf16x8, a), __builtin_bit_cast(bf16x8, b), c, 0, 0, 0);
}
template <int CTRL> __device__ __forceinline__ float dppf(float x) {
  return __builtin_bit_cast(float, __builtin_amdgcn_mov_dpp(__builtin_bit_cast(int, x), CTRL, 0xf, 0xf, true));
}
template <int CTRL> __device__ __forceinline__ unsigned dppu(unsigned x) {
  return (unsigned)__builtin_amdgcn_mov_dpp((int)x, CTRL, 0xf, 0xf, true);
}
constexpr int XOR1 = 0xB1, XOR2 = 0x4E, XOR7 = 0x141, XOR8 = 0x128, BCAST = 0x150;
__device__ __forceinline__ float xrow_sum(float x) {
  auto s = __builtin_amdgcn_permlane16_swap(__float_as_uint(x), __float_as_uint(x), false, false);
  x = __uint_as_float(s[0]) + __uint_as_float(s[1]);
  auto t = __builtin_amdgcn_permlane32_swap(__float_as_uint(x), __float_as_uint(x), false, false);
  return __uint_as_float(t[0]) + __uint_as_float(t[1]);
}
__device__ __forceinline__ float xrow_max(float x) {
  auto s = __builtin_amdgcn_permlane16_swap(__float_as_uint(x), __float_as_uint(x), false, false);
  x = fmaxf(__uint_as_float(s[0]), __uint_as_float(s[1]));
  auto t = __builtin_amdgcn_permlane32_swap(__float_as_uint(x), __float_as_uint(x), false, false);
  return fmaxf(__uint_as_float(t[0]), __uint_as_float(t[1]));
}
__device__ __forceinline__ float wave_sum(float x) {
  x += dppf<XOR1>(x); x += dppf<XOR2>(x); x += dppf<XOR7>(x); x += dppf<XOR8>(x);
  return xrow_sum(x);
}
__device__ __forceinline__ float wave_max(float x) {
  x = fmaxf(x, dppf<XOR1>(x)); x = fmaxf(x, dppf<XOR2>(x)); x = fmaxf(x, dppf<XOR7>(x)); x = fmaxf(x, dppf<XOR8>(x));
  return xrow_max(x);
}
#define WG_BARRIER() asm volatile("s_waitcnt lgkmcnt(0)\n\ts_barrier" ::: "memory")
#define SCHED_FENCE() __builtin_amdgcn_sched_barrier(0)

constexpr float SCL = 0.088388347648318440f * 1.4426950408889634f;

struct Soft { float m, mc, l; f32x4 acc; };

template <bool ISSUE>
__device__ __forceinline__ void tile_step(v4u (&kr)[4], v4u (&vr)[4], const GAS v4u* kn, const GAS v4u* vn, const v4u q,
                                          const unsigned (&mask)[4], bool hi8, bool hi4, Soft& s) {
  float x[4];
#pragma unroll
  for (int t = 0; t < 4; ++t) {
    float d = dot2(kr[t][0], q[0], 0.f);
    d = dot2(kr[t][1], q[1], d); d = dot2(kr[t][2], q[2], d); d = dot2(kr[t][3], q[3], d);
    x[t] = d;
    if (ISSUE) { asm volatile("" : "+v"(x[t])); kr[t] = ld_nt(kn + t * 64); SCHED_FENCE(); }
  }
  const float a0 = x[0] + dppf<XOR8>(x[0]), a2 = x[2] + dppf<XOR8>(x[2]);
  const float a1 = x[1] + dppf<XOR8>(x[1]), a3 = x[3] + dppf<XOR8>(x[3]);
  const float y0 = hi8 ? a2 : a0, y1 = hi8 ? a3 : a1;
  const float b0 = y0 + dppf<XOR7>(y0), b1 = y1 + dppf<XOR7>(y1);
  float z = hi4 ? b1 : b0;
  z += dppf<XOR2>(z); z += dppf<XOR1>(z);
  if (__builtin_amdgcn_ballot_w64(z > s.m) != 0) {
    const float mn = fmaxf(s.m, wave_max(z));
    const float mcn = mn * SCL;
    const float alpha = __builtin_amdgcn_exp2f(s.mc - mcn);
    s.m = mn; s.mc = mcn; s.l *= alpha; s.acc *= alpha;
  }
  const float p = __builtin_amdgcn_exp2f(fmaf(z, SCL, -s.mc));
  s.l += p;
  const unsigned pk = pk_bf16(p, p);
#define PV_STEP(t) { const v4u bf = {dppu<BCAST + 4 * t>(pk) & mask[0], dppu<BCAST + 4 * t>(pk) & mask[1], dppu<BCAST + 4 * t>(pk) & mask[2], dppu<BCAST + 4 * t>(pk) & mask[3]}; \
    s.acc = mfma(vr[t], bf, s.acc); if (ISSUE) { vr[t] = ld_nt(vn + t * 64); SCHED_FENCE(); } }
  PV_STEP(0) PV_STEP(1) PV_STEP(2) PV_STEP(3)
#undef PV_STEP
}

__global__ void __launch_bounds__(NTHR, 3) fused_kernel(Args a) {
  extern __shared__ __attribute__((aligned(16))) unsigned char smem[];
  LAS unsigned char* L = (LAS unsigned char*)smem;
  const int tid = threadIdx.x, lane = tid & 63, w = __builtin_amdgcn_readfirstlane(tid >> 6);
  const int bid = blockIdx.x;
  const int h = (bid & 7) * 4 + ((bid >> 3) & 3), b = bid >> 5;
  const int g = lane >> 4, c = lane & 15;

  if (w < NWS) {
    const GAS unsigned* xp = (const GAS unsigned*)(a.x + b * DM + 72 * w + 18 * g);
    unsigned xr[9];
#pragma unroll
    for (int i = 0; i < 9; ++i) xr[i] = xp[i];
    const GAS v4u* wqp = (const GAS v4u*)(a.wq + (size_t)(72 * w + 18 * g) * (NH * DH) + h * DH + 8 * c);
    v4u wr[18];
#pragma unroll
    for (int i = 0; i < 18; ++i) wr[i] = ld_w(wqp + i * (NH * DH / 8));
    SCHED_FENCE();
    const size_t kvoff = ((size_t)(b * NH + h) * SK + w * TK) * DH;
    const GAS v4u* kp = (const GAS v4u*)(a.k + kvoff) + lane;
    const GAS v4u* vp = (const GAS v4u*)(a.v + kvoff) + lane;
    constexpr int TSTR = NWS * TK * DH / 8;
    v4u kr[DEPTH][4], vr[DEPTH][4];
#pragma unroll
    for (int d = 0; d < DEPTH; ++d) {
#pragma unroll
      for (int t = 0; t < 4; ++t) { kr[d][t] = ld_nt(kp + d * TSTR + t * 64); SCHED_FENCE(); }
#pragma unroll
      for (int t = 0; t < 4; ++t) { vr[d][t] = ld_nt(vp + d * TSTR + t * 64); SCHED_FENCE(); }
    }
    unsigned mask[4];
#pragma unroll
    for (int r = 0; r < 4; ++r) mask[r] = (c >> 1) == r ? ((c & 1) ? 0xffff0000u : 0x0000ffffu) : 0u;
    f32x4 qa = {0.f, 0.f, 0.f, 0.f};
#pragma unroll
    for (int i = 0; i < 18; ++i) {
      const unsigned u = xr[i >> 1];
      const unsigned dup = __builtin_amdgcn_perm(u, u, (i & 1) ? 0x03020302u : 0x01000100u);
      const v4u bf = {dup & mask[0], dup & mask[1], dup & mask[2], dup & mask[3]};
      qa = mfma(wr[i], bf, qa);
    }
    LAS float* part = (LAS float*)(L + L_PART);
    if (c < 8) {
#pragma unroll
      for (int r = 0; r < 4; ++r) part[w * DH + 8 * (4 * g + r) + c] = qa[r];
    }
    WG_BARRIER();
    v4u q;
    {
      f32x4 s0 = {0.f, 0.f, 0.f, 0.f}, s1 = {0.f, 0.f, 0.f, 0.f};
#pragma unroll
      for (int i = 0; i < NWS; ++i) {
        s0 += *(const LAS f32x4*)(part + i * DH + 8 * c);
        s1 += *(const LAS f32x4*)(part + i * DH + 8 * c + 4);
      }
      q = v4u{pk_bf16(s0[0], s0[1]), pk_bf16(s0[2], s0[3]), pk_bf16(s1[0], s1[1]), pk_bf16(s1[2], s1[3])};
    }
    WG_BARRIER();
    Soft s; s.m = -INFINITY; s.mc = -INFINITY; s.l = 0.f; s.acc = f32x4{0.f, 0.f, 0.f, 0.f};
    const bool hi8 = (c & 8) != 0, hi4 = (c & 4) != 0;
#pragma unroll 1
    for (int it = 0; it < NT / DEPTH - 1; ++it) {
      kp += DEPTH * TSTR; vp += DEPTH * TSTR;
#pragma unroll
      for (int d = 0; d < DEPTH; ++d) tile_step<true>(kr[d], vr[d], kp + d * TSTR, vp + d * TSTR, q, mask, hi8, hi4, s);
    }
#pragma unroll
    for (int d = 0; d < DEPTH; ++d) tile_step<false>(kr[d], vr[d], kp, vp, q, mask, hi8, hi4, s);
    const float lsum = wave_sum(s.l) * 0.25f;
    if (c < 8) {
#pragma unroll
      for (int r = 0; r < 4; ++r) part[w * DH + 8 * (4 * g + r) + c] = s.acc[r];
    }
    if (lane == 0) { ((LAS float*)(L + L_ML))[2 * w] = s.mc; ((LAS float*)(L + L_ML))[2 * w + 1] = lsum; }
  } else if (w < NWS + 3) {
    const int m = w - NWS;
    const GAS u16* xp = (const GAS u16*)(a.x + b * DM);
    float xv[9];
#pragma unroll
    for (int i = 0; i < 9; ++i) xv[i] = bf2f(xp[lane + 64 * i]);
    const int l48 = lane < 48 ? lane : lane - 48, rg = l48 / 6, cc = l48 % 6;
    const GAS v4u* w1p = (const GAS v4u*)(a.w1 + (size_t)(72 * rg) * FF + FS * h + 48 * m + 8 * cc);
    const float b1v = bf2f(((const GAS u16*)a.b1)[FS * h + 48 * m + l48]);
    v4u wr[24];
#pragma unroll
    for (int i = 0; i < 24; ++i) wr[i] = ld_w(w1p + i * (FF / 8));
    SCHED_FENCE();
    WG_BARRIER();
    WG_BARRIER();
    float sum = 0.f;
#pragma unroll
    for (int i = 0; i < 9; ++i) sum += xv[i];
    const float mean = wave_sum(sum) * (1.f / DM);
    float sq = 0.f;
#pragma unroll
    for (int i = 0; i < 9; ++i) sq += (xv[i] - mean) * (xv[i] - mean);
    const float var = rbf(wave_sum(sq) * (1.f / DM));
    const float mb = rbf(mean), sd = rbf(sqrtf(rbf(var + 1e-5f)));
    LAS u16* yl = (LAS u16*)(L + L_Y + m * 1152);
#pragma unroll
    for (int i = 0; i < 9; ++i) yl[lane + 64 * i] = __builtin_bit_cast(u16, (__bf16)(rbf(xv[i] - mb) / sd));
    asm volatile("s_waitcnt lgkmcnt(0)" ::: "memory");
    float acc[8];
#pragma unroll
    for (int e = 0; e < 8; ++e) acc[e] = 0.f;
#define W1_BATCH(bt, REFILL) { \
      v4u yy[3]; \
      _Pragma("unroll") for (int i = 0; i < 3; ++i) yy[i] = *(const LAS v4u*)(yl + 72 * rg + 24 * (bt) + 8 * i); \
      _Pragma("unroll") for (int i = 0; i < 24; ++i) { \
        const unsigned u = yy[i >> 3][(i >> 1) & 3]; \
        const float yv = (i & 1) ? bfhi(u) : bflo(u); \
        _Pragma("unroll") for (int e = 0; e < 4; ++e) { \
          acc[2 * e] = fmaf(yv, bflo(wr[i][e]), acc[2 * e]); \
          acc[2 * e + 1] = fmaf(yv, bfhi(wr[i][e]), acc[2 * e + 1]); \
        } \
        if (REFILL) { asm volatile("" : "+v"(acc[0]), "+v"(acc[1]), "+v"(acc[2]), "+v"(acc[3]), "+v"(acc[4]), "+v"(acc[5]), "+v"(acc[6]), "+v"(acc[7])); \
          wr[i] = ld_w(w1p + (size_t)(24 * ((bt) + 1) + i) * (FF / 8)); SCHED_FENCE(); } \
      } }
#pragma unroll 1
    for (int bt = 0; bt < 2; ++bt) W1_BATCH(bt, true)
    W1_BATCH(2, false)
#undef W1_BATCH
    LAS float* mp = (LAS float*)(L + L_MLP + m * 2304);
    if (lane < 48) {
      *(LAS f32x4*)(mp + rg * 48 + 8 * cc) = f32x4{acc[0], acc[1], acc[2], acc[3]};
      *(LAS f32x4*)(mp + rg * 48 + 8 * cc + 4) = f32x4{acc[4], acc[5], acc[6], acc[7]};
    }
    asm volatile("s_waitcnt lgkmcnt(0)" ::: "memory");
    float hs = 0.f;
#pragma unroll
    for (int i = 0; i < 8; ++i) hs += mp[i * 48 + l48];
    const float pre = rbf(rbf(hs) + b1v);
    const float inner = 0.7978845608028654f * (pre + 0.044715f * pre * pre * pre);
    const float th = 1.f - 2.f / (1.f + __expf(2.f * inner));
    const float hv = rbf(0.5f * pre * (1.f + th));
    LAS float* hid = (LAS float*)(L + L_HID + m * 192);
    asm volatile("s_waitcnt lgkmcnt(0)" ::: "memory");
    if (lane < 48) hid[lane] = hv;
#pragma unroll
    for (int i = 0; i < 9; ++i) mp[lane + 64 * i] = 0.f;
    asm volatile("s_waitcnt lgkmcnt(0)" ::: "memory");
    const GAS v4u* w2p = (const GAS v4u*)(a.w2 + (size_t)(FS * h + 48 * m) * DM) + lane;
#pragma unroll 1
    for (int j3 = 0; j3 < 3; ++j3) {
      v4u w2r[18];
#pragma unroll
      for (int i = 0; i < 18; ++i) w2r[i] = ld_w(w2p + (3 * j3 + i / 6) * 64 + (i % 6) * 576);
      SCHED_FENCE();
#pragma unroll
      for (int jj = 0; jj < 3; ++jj) {
        const int idx = (3 * j3 + jj) * 64 + lane, rr = idx / 72, c2 = idx % 72;
        float ac[8];
#pragma unroll
        for (int e = 0; e < 8; ++e) ac[e] = 0.f;
#pragma unroll
        for (int gr = 0; gr < 6; ++gr) {
          const float hvv = hid[8 * gr + rr];
#pragma unroll
          for (int e = 0; e < 4; ++e) {
            ac[2 * e] = fmaf(hvv, bflo(w2r[jj * 6 + gr][e]), ac[2 * e]);
            ac[2 * e + 1] = fmaf(hvv, bfhi(w2r[jj * 6 + gr][e]), ac[2 * e + 1]);
          }
        }
#pragma unroll
        for (int e = 0; e < 8; ++e) (void)__hip_atomic_fetch_add(mp + 8 * c2 + e, ac[e], __ATOMIC_RELAXED, __HIP_MEMORY_SCOPE_WORKGROUP);
      }
    }
  } else {
    const GAS u16* xp = (const GAS u16*)(a.x + b * DM);
    const GAS u16* gp = (const GAS u16*)a.gw;
    unsigned xv[9], gv[9];
#pragma unroll
    for (int i = 0; i < 9; ++i) { xv[i] = xp[lane + 64 * i]; gv[i] = gp[lane + 64 * i]; }
    const u64 ep = __hip_atomic_load((GAS u64*)(a.ws + W_EPOCH + 256 * b), __ATOMIC_RELAXED, __HIP_MEMORY_SCOPE_AGENT);
    WG_BARRIER();
    WG_BARRIER();
    float sum = 0.f;
#pragma unroll
    for (int i = 0; i < 9; ++i) sum += rbf(bf2f(xv[i]) * bf2f(gv[i]));
    const float logit = rbf(wave_sum(sum));
    const float gate = rbf(1.f / (1.f + __expf(-logit)));
    if (lane == 0) { *(LAS float*)(L + L_MISC) = gate; *(LAS u64*)(L + L_MISC + 8) = ep + 1; }
    const GAS unsigned char* wob = (const GAS unsigned char*)(a.wo + (size_t)h * DH * DM);
    int so[9];
#pragma unroll
    for (int j = 0; j < 9; ++j) {
      const int sl = 64 * j + lane, row = sl / 72, ch = sl % 72;
      so[j] = row * 1152 + ((((ch >> 1) ^ ((row >> 1) & 3)) << 1) | (ch & 1)) * 16;
    }
#pragma unroll 1
    for (int gr = 0; gr < 16; ++gr) {
#pragma unroll
      for (int j = 0; j < 9; ++j)
        __builtin_amdgcn_global_load_lds((const GAS void*)(wob + gr * 9216 + so[j]), (LAS void*)(L + L_WO + gr * 9216 + j * 1024), 16, 0, GM_WNT ? 2 : 0);
      asm volatile("s_waitcnt vmcnt(27)" ::: "memory");
    }
    asm volatile("s_waitcnt vmcnt(0)" ::: "memory");
  }
  WG_BARRIER();
  if (tid < DH) {
    const LAS float* ml = (const LAS float*)(L + L_ML);
    const LAS float* part = (const LAS float*)(L + L_PART);
    float mx = ml[0];
#pragma unroll
    for (int i = 1; i < NWS; ++i) mx = fmaxf(mx, ml[2 * i]);
    float den = 0.f, num = 0.f;
#pragma unroll
    for (int i = 0; i < NWS; ++i) {
      const float f = __builtin_amdgcn_exp2f(ml[2 * i] - mx);
      den = fmaf(f, ml[2 * i + 1], den); num = fmaf(f, part[i * DH + tid], num);
    }
    const int pos = (tid & 96) + 8 * ((tid >> 2) & 3) + 4 * ((tid >> 4) & 1) + (tid & 3);
    ((LAS u16*)(L + L_OBF))[pos] = __builtin_bit_cast(u16, (__bf16)(num / den));
  }
  WG_BARRIER();
  const float gate = *(const LAS float*)(L + L_MISC);
  const float omg = rbf(1.f - gate);
  const u64 ep1 = *(const LAS u64*)(L + L_MISC + 8);
  const u64 tag = ep1 & ((1ull << 40) - 1);
  {
    const int q4 = (lane >> 2) & 3, p4 = lane & 3;
    const int base = (4 * g + q4) * 1152 + ((w ^ ((2 * g + (q4 >> 1)) & 3)) * 32) + 8 * p4;
    f32x4 oc[3];
#pragma unroll
    for (int j = 0; j < 3; ++j) oc[j] = f32x4{0.f, 0.f, 0.f, 0.f};
#pragma unroll
    for (int ks = 0; ks < 4; ++ks) {
      const v4u af = *(const LAS v4u*)(L + L_OBF + 64 * ks + 16 * g);
#pragma unroll
      for (int j = 0; j < 3; ++j) {
        const s16x4 lo = __builtin_amdgcn_ds_read_tr16_b64_v4i16((LAS s16x4*)(L + L_WO + base + ks * 32 * 1152 + j * 384));
        const s16x4 hi = __builtin_amdgcn_ds_read_tr16_b64_v4i16((LAS s16x4*)(L + L_WO + base + (ks * 32 + 16) * 1152 + j * 384));
        const v2u l2 = __builtin_bit_cast(v2u, lo), h2 = __builtin_bit_cast(v2u, hi);
        oc[j] = mfma(af, v4u{l2[0], l2[1], h2[0], h2[1]}, oc[j]);
      }
    }
    if (lane < 48) {
      const int cb = w + 12 * g, n = 16 * cb + c;
      const float av = g == 0 ? oc[0][0] : (g == 1 ? oc[1][0] : oc[2][0]);
      const LAS float* mp = (const LAS float*)(L + L_MLP);
      const float tot = gate * av + omg * ((mp[n] + mp[DM + n]) + mp[2 * DM + n]);
      const u64 gr = (tag << 24) | (u64)((__builtin_bit_cast(unsigned, tot) + 0x80u) >> 8);
      __hip_atomic_store((GAS u64*)(a.ws + W_INBOX) + ((size_t)(b * 36 + cb) * 32 + h) * 16 + c, gr, __ATOMIC_RELAXED, __HIP_MEMORY_SCOPE_AGENT);
    }
  }
  if (w == 0 || (w == 1 && h < 4)) {
    const int cb = h + 32 * w;
    const float b2v = bf2f(((const GAS u16*)a.b2)[16 * cb + c]);
    GAS u64* ib = (GAS u64*)(a.ws + W_INBOX) + (size_t)(b * 36 + cb) * 32 * 16 + lane;
    u64 gv[8];
#pragma unroll
    for (int i = 0; i < 8; ++i) gv[i] = __hip_atomic_load(ib + 64 * i, __ATOMIC_RELAXED, __HIP_MEMORY_SCOPE_AGENT);
    for (unsigned spin = 0; spin < (1u << 22); ++spin) {
      bool all = true;
#pragma unroll
      for (int i = 0; i < 8; ++i) all = all && (gv[i] >> 24) == tag;
      if (__builtin_amdgcn_ballot_w64(!all) == 0) break;
      __builtin_amdgcn_s_sleep(2);
#pragma unroll
      for (int i = 0; i < 8; ++i)
        if ((gv[i] >> 24) != tag) gv[i] = __hip_atomic_load(ib + 64 * i, __ATOMIC_RELAXED, __HIP_MEMORY_SCOPE_AGENT);
    }
    float sum = 0.f;
#pragma unroll
    for (int i = 0; i < 8; ++i) sum += __builtin_bit_cast(float, (unsigned)gv[i] << 8);
    sum = xrow_sum(sum);
    if (lane < 16) ((GAS u16*)a.out)[b * DM + 16 * cb + c] = __builtin_bit_cast(u16, (__bf16)(sum + omg * b2v));
    if (w == 0 && h == 0 && lane == 0)
      __hip_atomic_store((GAS u64*)(a.ws + W_EPOCH + 256 * b), ep1, __ATOMIC_RELAXED, __HIP_MEMORY_SCOPE_AGENT);
  }
}

}

extern "C" void kernel_launch(void* const* d_in, const int* in_sizes, int n_in, void* d_out, int out_size, void* d_ws,
                              size_t ws_size, hipStream_t stream) {
  (void)in_sizes; (void)n_in; (void)out_size; (void)ws_size;
  gm::Args a;
  a.x = (const gm::u16*)d_in[0]; a.k = (const gm::u16*)d_in[1]; a.v = (const gm::u16*)d_in[2];
  a.wq = (const gm::u16*)d_in[3]; a.wo = (const gm::u16*)d_in[4];
  a.w1 = (const gm::u16*)d_in[6]; a.b1 = (const gm::u16*)d_in[7]; a.w2 = (const gm::u16*)d_in[8]; a.b2 = (const gm::u16*)d_in[9];
  a.gw = (const gm::u16*)d_in[10];
  a.out = (gm::u16*)d_out; a.ws = (unsigned char*)d_ws;
  (void)hipFuncSetAttribute((const void*)gm::fused_kernel, hipFuncAttributeMaxDynamicSharedMemorySize, gm::L_BYTES);
  hipLaunchKernelGGL(gm::fused_kernel, dim3(gm::NB * gm::NH), dim3(gm::NTHR), gm::L_BYTES, stream, a);
}
```

```cpp
#include <hip/hip_runtime.h>
#include <hip/hip_bf16.h>
#include <stdint.h>

namespace gm {

constexpr int NB = 8, NH = 32, SK = 4096, DH = 128, DM = 576, FF = 4608;
constexpr int FS = FF / NH;
constexpr int NWS = 8, NWA = 4, NTHR = (NWS + NWA) * 64;
constexpr int TK = 16;
constexpr int NT = SK / TK / NWS;
#define GM_DEPTH 3
constexpr int DEPTH = GM_DEPTH;
#define GM_WNT 1
#define GM_PRIO 1
#define GM_LATE 1
#define GM_B0 1
#define GM_AW 2
#define STR2(x) #x
#define STR(x) STR2(x)
#define GM_SW 2
#define GM_SW0 GM_SW
#define KV_THROTTLE() asm volatile("s_waitcnt vmcnt(" STR(GM_SW) ")" ::: "memory")
#define KV_THROTTLE0() asm volatile("s_waitcnt vmcnt(" STR(GM_SW0) ")" ::: "memory")
#define GM_ROT 0
#define GM_R1 3
#define AUX_THROTTLE() asm volatile("s_waitcnt vmcnt(" STR(GM_AW) ")" ::: "memory")

constexpr int L_WO = 0;
constexpr int L_PART = 128 * 1152;
constexpr int L_MLP = L_PART + 4096;
constexpr int L_Y = L_MLP + 3 * 2304;
constexpr int L_HID = L_Y + 3 * 1152;
constexpr int L_ML = L_HID + 3 * 192;
constexpr int L_OBF = L_ML + 64;
constexpr int L_Q = L_OBF + 256;
constexpr int L_MISC = L_Q + 256;
constexpr int L_BYTES = L_MISC + 16;
static_assert(L_BYTES <= 163840, "LDS");

constexpr size_t W_EPOCH = 0;
constexpr size_t W_QX = 4096;
constexpr size_t W_INBOX = W_QX + (size_t)NH * NB * 4 * 128 * 8;
constexpr size_t W_BYTES = W_INBOX + (size_t)NB * 36 * 32 * 16 * 8;

#define GAS __attribute__((address_space(1)))
#define LAS __attribute__((address_space(3)))
typedef unsigned v4u __attribute__((ext_vector_type(4)));
typedef unsigned v2u __attribute__((ext_vector_type(2)));
typedef float f32x4 __attribute__((ext_vector_type(4)));
typedef short s16x4 __attribute__((ext_vector_type(4)));
typedef __bf16 bf16x2 __attribute__((ext_vector_type(2)));
typedef __bf16 bf16x8 __attribute__((ext_vector_type(8)));
typedef unsigned long long u64;
typedef unsigned short u16;

struct Args {
  const u16* x; const u16* k; const u16* v; const u16* wq; const u16* wo;
  const u16* w1; const u16* b1; const u16* w2; const u16* b2; const u16* gw;
  u16* out; unsigned char* ws;
};

__device__ __forceinline__ v4u ld_nt(const GAS v4u* p) { return __builtin_nontemporal_load(p); }
__device__ __forceinline__ v4u ld_w(const GAS v4u* p) {
  return __builtin_nontemporal_load(p);
}
__device__ __forceinline__ void glds16(const GAS unsigned char* g, unsigned lds_base) {
  unsigned keep;
  asm volatile("s_mov_b32 %0, m0\n\ts_mov_b32 m0, %2\n\ts_nop 0\n\tglobal_load_lds_dwordx4 %1, off nt\n\ts_mov_b32 m0, %0" : "=&s"(keep) : "v"(g), "s"(lds_base) : "memory");
}
__device__ __forceinline__ float bf2f(unsigned h) { return __builtin_bit_cast(float, h << 16); }
__device__ __forceinline__ float bflo(unsigned u) { return __builtin_bit_cast(float, u << 16); }
__device__ __forceinline__ float bfhi(unsigned u) { return __builtin_bit_cast(float, u & 0xffff0000u); }
__device__ __forceinline__ unsigned pk_bf16(float lo, float hi) {
  bf16x2 p = {(__bf16)lo, (__bf16)hi};
  return __builtin_bit_cast(unsigned, p);
}
__device__ __forceinline__ float rbf(float f) { return (float)(__bf16)f; }
__device__ __forceinline__ float dot2(unsigned a, unsigned b, float c) {
  return __builtin_amdgcn_fdot2_f32_bf16(__builtin_bit_cast(bf16x2, a), __builtin_bit_cast(bf16x2, b), c, false);
}
__device__ __forceinline__ f32x4 mfma(v4u a, v4u b, f32x4 c) {
  return __builtin_amdgcn_mfma_f32_16x16x32_bf16(__builtin_bit_cast(bf16x8, a), __builtin_bit_cast(bf16x8, b), c, 0, 0, 0);
}
template <int CTRL> __device__ __forceinline__ float dppf(float x) {
  return __builtin_bit_cast(float, __builtin_amdgcn_mov_dpp(__builtin_bit_cast(int, x), CTRL, 0xf, 0xf, true));
}
template <int CTRL> __device__ __forceinline__ unsigned dppu(unsigned x) {
  return (unsigned)__builtin_amdgcn_mov_dpp((int)x, CTRL, 0xf, 0xf, true);
}
constexpr int XOR1 = 0xB1, XOR2 = 0x4E, XOR7 = 0x141, XOR8 = 0x128, BCAST = 0x150;
__device__ __forceinline__ float xrow_sum(float x) {
  auto s = __builtin_amdgcn_permlane16_swap(__float_as_uint(x), __float_as_uint(x), false, false);
  x = __uint_as_float(s[0]) + __uint_as_float(s[1]);
  auto t = __builtin_amdgcn_permlane32_swap(__float_as_uint(x), __float_as_uint(x), false, false);
  return __uint_as_float(t[0]) + __uint_as_float(t[1]);
}
__device__ __forceinline__ float xrow_max(float x) {
  auto s = __builtin_amdgcn_permlane16_swap(__float_as_uint(x), __float_as_uint(x), false, false);
  x = fmaxf(__uint_as_float(s[0]), __uint_as_float(s[1]));
  auto t = __builtin_amdgcn_permlane32_swap(__float_as_uint(x), __float_as_uint(x), false, false);
  return fmaxf(__uint_as_float(t[0]), __uint_as_float(t[1]));
}
__device__ __forceinline__ float fold_halves(float x) {
  const float t = dppf<XOR8>(x);
  auto p = __builtin_amdgcn_permlane32_swap(__float_as_uint(t), __float_as_uint(t), false, false);
  return x + __uint_as_float(p[1]);
}
__device__ __forceinline__ float wave_sum(float x) {
  x += dppf<XOR1>(x); x += dppf<XOR2>(x); x += dppf<XOR7>(x); x += dppf<XOR8>(x);
  return xrow_sum(x);
}
__device__ __forceinline__ float wave_max(float x) {
  x = fmaxf(x, dppf<XOR1>(x)); x = fmaxf(x, dppf<XOR2>(x)); x = fmaxf(x, dppf<XOR7>(x)); x = fmaxf(x, dppf<XOR8>(x));
  return xrow_max(x);
}
#define WG_BARRIER() asm volatile("s_waitcnt lgkmcnt(0)\n\ts_barrier" ::: "memory")
#define SCHED_FENCE() __builtin_amdgcn_sched_barrier(0)

#define STAMP(i) do { } while (0)

constexpr float SCL = 0.088388347648318440f * 1.4426950408889634f;

#define GVAL(x) __builtin_bit_cast(float, (unsigned)(x) << 8)
__device__ __forceinline__ void gather8(GAS u64* ib, u64 tag, u64 (&gv)[8]) {
#pragma unroll
  for (int i = 0; i < 8; ++i) gv[i] = __hip_atomic_load(ib + 64 * i, __ATOMIC_RELAXED, __HIP_MEMORY_SCOPE_AGENT);
  for (unsigned spin = 0; spin < (1u << 22); ++spin) {
    bool all = true;
#pragma unroll
    for (int i = 0; i < 8; ++i) all = all && (gv[i] >> 24) == tag;
    if (__builtin_amdgcn_ballot_w64(!all) == 0) break;
    __builtin_amdgcn_s_sleep(1);
#pragma unroll
    for (int i = 0; i < 8; ++i)
      if ((gv[i] >> 24) != tag) gv[i] = __hip_atomic_load(ib + 64 * i, __ATOMIC_RELAXED, __HIP_MEMORY_SCOPE_AGENT);
  }
}

struct Soft { float m, mc, l; f32x4 acc; };

template <bool ISSUE>
__device__ __forceinline__ void tile_step(v4u (&kr)[4], v4u (&vr)[4], const GAS v4u* kn, const GAS v4u* vn, const v4u q,
                                          const unsigned (&mask)[4], bool hi8, bool hi4, Soft& s) {
  float x[4];
#pragma unroll
  for (int t = 0; t < 4; ++t) {
    float d = dot2(kr[t][0], q[0], 0.f);
    d = dot2(kr[t][1], q[1], d); d = dot2(kr[t][2], q[2], d); d = dot2(kr[t][3], q[3], d);
    x[t] = d;
    if (ISSUE) { asm volatile("" : "+v"(x[t])); KV_THROTTLE(); kr[t] = ld_nt(kn + t * 64); SCHED_FENCE(); }
  }
  const float a0 = x[0] + dppf<XOR8>(x[0]), a2 = x[2] + dppf<XOR8>(x[2]);
  const float a1 = x[1] + dppf<XOR8>(x[1]), a3 = x[3] + dppf<XOR8>(x[3]);
  const float y0 = hi8 ? a2 : a0, y1 = hi8 ? a3 : a1;
  const float b0 = y0 + dppf<XOR7>(y0), b1 = y1 + dppf<XOR7>(y1);
  float z = hi4 ? b1 : b0;
  z += dppf<XOR2>(z); z += dppf<XOR1>(z);
  if (__builtin_amdgcn_ballot_w64(z > s.m) != 0) {
    const float mn = fmaxf(s.m, wave_max(z));
    const float mcn = mn * SCL;
    const float alpha = __builtin_amdgcn_exp2f(s.mc - mcn);
    s.m = mn; s.mc = mcn; s.l *= alpha; s.acc *= alpha;
  }
  const float p = __builtin_amdgcn_exp2f(fmaf(z, SCL, -s.mc));
  s.l += p;
  const unsigned pk = pk_bf16(p, p);
#define PV_STEP(t) { const v4u bf = {dppu<BCAST + 4 * t>(pk) & mask[0], dppu<BCAST + 4 * t>(pk) & mask[1], dppu<BCAST + 4 * t>(pk) & mask[2], dppu<BCAST + 4 * t>(pk) & mask[3]}; \
    s.acc = mfma(vr[t], bf, s.acc); if (ISSUE) { SCHED_FENCE(); KV_THROTTLE(); vr[t] = ld_nt(vn + t * 64); SCHED_FENCE(); } }
  PV_STEP(0) PV_STEP(1) PV_STEP(2) PV_STEP(3)
#undef PV_STEP
}

__global__ void __launch_bounds__(NTHR, 3) fused_kernel(Args a) {
  extern __shared__ __attribute__((aligned(16))) unsigned char smem[];
  LAS unsigned char* L = (LAS unsigned char*)smem;
  const int tid = threadIdx.x, lane = tid & 63, w = __builtin_amdgcn_readfirstlane(tid >> 6);
  const int bid = blockIdx.x;
  const int h = (bid & 7) * 4 + ((bid >> 3) & 3), b = bid >> 5;
  const int g = lane >> 4, c = lane & 15;

  if (w >= NWA) {
    const int ws_ = w - NWA;
    const size_t kvoff = ((size_t)(b * NH + h) * SK + ws_ * TK) * DH;
    const GAS v4u* kp = (const GAS v4u*)(a.k + kvoff) + lane;
    const GAS v4u* vp = (const GAS v4u*)(a.v + kvoff) + lane;
    constexpr int TSTR = NWS * TK * DH / 8;
    v4u kr[DEPTH][4], vr[DEPTH][4];
    int rot = GM_ROT == 0 ? 0 : GM_ROT == 1 ? (int)((bid * 2654435761u) >> 27) : GM_ROT == 2 ? (int)(((bid * 8 + ws_) * 2654435761u) >> 27) : GM_ROT == 3 ? (bid >> 5) * 4 : GM_ROT == 4 ? ws_ * 4 : (bid >> 3);
#define TOFF(i) ((((i) + rot) & (NT - 1)) * TSTR)
    if (GM_B0) WG_BARRIER();
#pragma unroll
    for (int d = 0; d < DEPTH; ++d) {
#pragma unroll
      for (int t = 0; t < 4; ++t) { KV_THROTTLE0(); kr[d][t] = ld_nt(kp + TOFF(d) + t * 64); SCHED_FENCE(); }
#pragma unroll
      for (int t = 0; t < 4; ++t) { KV_THROTTLE0(); vr[d][t] = ld_nt(vp + TOFF(d) + t * 64); SCHED_FENCE(); }
    }
    unsigned mask[4];
#pragma unroll
    for (int r = 0; r < 4; ++r) mask[r] = (c >> 1) == r ? ((c & 1) ? 0xffff0000u : 0x0000ffffu) : 0u;
    LAS float* part = (LAS float*)(L + L_PART);
    WG_BARRIER();
    const v4u q = *(const LAS v4u*)(L + L_Q + 16 * c);
    Soft s; s.m = -INFINITY; s.mc = -INFINITY; s.l = 0.f; s.acc = f32x4{0.f, 0.f, 0.f, 0.f};
    const bool hi8 = (c & 8) != 0, hi4 = (c & 4) != 0;
    constexpr int REM = NT % DEPTH;
#pragma unroll 1
    for (int it = 0; it < NT / DEPTH - 1; ++it) {
      rot += DEPTH;
#pragma unroll
      for (int d = 0; d < DEPTH; ++d) tile_step<true>(kr[d], vr[d], kp + TOFF(d), vp + TOFF(d), q, mask, hi8, hi4, s);
    }
    rot += DEPTH;
#pragma unroll
    for (int d = 0; d < DEPTH; ++d) {
      if (d < REM) tile_step<true>(kr[d], vr[d], kp + TOFF(d), vp + TOFF(d), q, mask, hi8, hi4, s);
      else tile_step<false>(kr[d], vr[d], kp, vp, q, mask, hi8, hi4, s);
    }
#pragma unroll
    for (int d = 0; d < REM; ++d) tile_step<false>(kr[d], vr[d], kp, vp, q, mask, hi8, hi4, s);
    const float lsum = wave_sum(s.l) * 0.25f;
    if (c < 8) {
#pragma unroll
      for (int r = 0; r < 4; ++r) part[ws_ * DH + 8 * (4 * g + r) + c] = s.acc[r];
    }
    if (lane == 0) { ((LAS float*)(L + L_ML))[2 * ws_] = s.mc; ((LAS float*)(L + L_ML))[2 * ws_ + 1] = lsum; }
  } else if (w > 0) {
    const int m = w - 1;
    if (GM_PRIO) __builtin_amdgcn_s_setprio(3);
    if (GM_B0) WG_BARRIER();
    WG_BARRIER();
    const GAS u16* xp = (const GAS u16*)(a.x + b * DM);
    float xv[9];
#pragma unroll
    for (int i = 0; i < 9; ++i) xv[i] = bf2f(xp[lane + 64 * i]);
    const int col0 = 64 * ((9 * h) / 4 + m), sl = 4 * (c >> 3) + g;
    const GAS unsigned char* w1p = (const GAS unsigned char*)a.w1 + (size_t)(72 * sl) * (FF * 2) + col0 * 2 + (c & 7) * 16;
    constexpr int R1 = GM_R1;
    v4u r1[R1];
#pragma unroll
    for (int i = 0; i < R1; ++i) { r1[i] = ld_w((const GAS v4u*)(w1p + i * (FF * 2))); SCHED_FENCE(); }
    float b1v[4];
#pragma unroll
    for (int r = 0; r < 4; ++r) b1v[r] = bf2f(((const GAS u16*)a.b1)[col0 + 8 * (4 * (g & 1) + r) + (c & 7)]);
    if (m == 0) {
      const GAS u16* gp = (const GAS u16*)a.gw;
      float gs = 0.f;
#pragma unroll
      for (int i = 0; i < 9; ++i) gs += rbf(xv[i] * bf2f(gp[lane + 64 * i]));
      const float logit = rbf(wave_sum(gs));
      const float gate = rbf(1.f / (1.f + __expf(-logit)));
      if (lane == 0) *(LAS float*)(L + L_MISC) = gate;
    }
    float sum = 0.f;
#pragma unroll
    for (int i = 0; i < 9; ++i) sum += xv[i];
    const float mean = wave_sum(sum) * (1.f / DM);
    float sq = 0.f;
#pragma unroll
    for (int i = 0; i < 9; ++i) sq += (xv[i] - mean) * (xv[i] - mean);
    const float var = rbf(wave_sum(sq) * (1.f / DM));
    const float mb = rbf(mean), sd = rbf(sqrtf(rbf(var + 1e-5f)));
    LAS u16* yl = (LAS u16*)(L + L_Y + m * 1152);
#pragma unroll
    for (int i = 0; i < 9; ++i) yl[lane + 64 * i] = __builtin_bit_cast(u16, (__bf16)(rbf(xv[i] - mb) / sd));
    asm volatile("s_waitcnt lgkmcnt(0)" ::: "memory");
    unsigned mask[4];
#pragma unroll
    for (int r = 0; r < 4; ++r) mask[r] = ((c & 7) >> 1) == r ? ((c & 1) ? 0xffff0000u : 0x0000ffffu) : 0u;
    f32x4 h1 = {0.f, 0.f, 0.f, 0.f};
    static_assert(24 % R1 == 0, "ring");
#pragma unroll 1
    for (int o = 0; o < 72; o += 24) {
      v4u yy[3];
#pragma unroll
      for (int i = 0; i < 3; ++i) yy[i] = *(const LAS v4u*)(yl + 72 * sl + o + 8 * i);
#pragma unroll
      for (int i = 0; i < 24; ++i) {
        const unsigned u = yy[i >> 3][(i >> 1) & 3];
        const unsigned dup = __builtin_amdgcn_perm(u, u, (i & 1) ? 0x03020302u : 0x01000100u);
        h1 = mfma(r1[i % R1], v4u{dup & mask[0], dup & mask[1], dup & mask[2], dup & mask[3]}, h1);
        SCHED_FENCE();
        const int nx = o + i + R1 < 72 ? o + i + R1 : 71;
        r1[i % R1] = ld_w((const GAS v4u*)(w1p + (size_t)nx * (FF * 2)));
        SCHED_FENCE();
      }
    }
    const int lo = col0 > FS * h ? col0 : FS * h, hi = col0 + 64 < FS * h + FS ? col0 + 64 : FS * h + FS;
    const int nrg = (hi - lo) >> 3;
    const GAS unsigned char* w2p = (const GAS unsigned char*)a.w2 + (size_t)(lo + sl) * (DM * 2) + (c & 7) * 16;
    v4u r2[3];
#pragma unroll
    for (int i = 0; i < 3; ++i) { r2[i] = ld_w((const GAS v4u*)(w2p + i * 128)); SCHED_FENCE(); }
    LAS u16* hid = (LAS u16*)(L + L_HID + m * 128);
#pragma unroll
    for (int r = 0; r < 4; ++r) {
      const float pre = rbf(rbf(fold_halves(h1[r])) + b1v[r]);
      const float inner = 0.7978845608028654f * (pre + 0.044715f * pre * pre * pre);
      const float th = 1.f - 2.f / (1.f + __expf(2.f * inner));
      if (lane < 32 && c < 8) hid[8 * (4 * g + r) + c] = __builtin_bit_cast(u16, (__bf16)(0.5f * pre * (1.f + th)));
    }
    asm volatile("s_waitcnt lgkmcnt(0)" ::: "memory");
    f32x4 o2[9];
#pragma unroll
    for (int j = 0; j < 9; ++j) o2[j] = f32x4{0.f, 0.f, 0.f, 0.f};
#pragma unroll 1
    for (int rg = 0; rg < nrg; ++rg) {
      const unsigned u = hid[lo - col0 + 8 * rg + sl];
      const unsigned dup = u | (u << 16);
      const v4u bf = {dup & mask[0], dup & mask[1], dup & mask[2], dup & mask[3]};
      const GAS unsigned char* cur = w2p + (size_t)rg * (8 * DM * 2);
      const GAS unsigned char* nxt = w2p + (size_t)(rg + 1 < nrg ? rg + 1 : rg) * (8 * DM * 2);
#pragma unroll
      for (int j = 0; j < 9; ++j) {
        o2[j] = mfma(r2[j % 3], bf, o2[j]);
        SCHED_FENCE();
        r2[j % 3] = ld_w((const GAS v4u*)(j < 6 ? cur + (j + 3) * 128 : nxt + (j - 6) * 128));
        SCHED_FENCE();
      }
    }
    LAS float* mp = (LAS float*)(L + L_MLP + m * 2304);
#pragma unroll
    for (int j = 0; j < 9; ++j)
#pragma unroll
      for (int r = 0; r < 4; ++r) {
        const float t = fold_halves(o2[j][r]);
        if (lane < 32 && c < 8) mp[64 * j + 8 * (4 * g + r) + c] = t;
      }
  } else {
    if (GM_PRIO) __builtin_amdgcn_s_setprio(3);
    {
      u64 e1 = *(const __attribute__((address_space(4))) u64*)(a.ws + W_EPOCH);
      const int ch = b & 1, kq = b >> 1;
      const GAS unsigned char* wqb = (const GAS unsigned char*)a.wq + (size_t)(144 * kq + 8 * (lane >> 5) + ((lane >> 3) & 3)) * (NH * DH * 2)
                                     + h * 256 + ch * 128 + (((((lane & 7) >> 1) ^ g) << 1) | (lane & 1)) * 16;
#pragma unroll
      for (int i = 0; i < 18; ++i) glds16(wqb + (size_t)(16 * (i >> 1) + 4 * (i & 1)) * (NH * DH * 2), (unsigned)(size_t)(L + L_WO) + i * 1024);
      const GAS unsigned char* xb = (const GAS unsigned char*)a.x + (c & 7) * (DM * 2) + 288 * kq;
      v4u xa[5];
#pragma unroll
      for (int ks = 0; ks < 4; ++ks) xa[ks] = *(const GAS v4u*)(xb + 64 * ks + 16 * g);
      xa[4] = *(const GAS v4u*)(xb + 256 + 16 * (g & 1));
      SCHED_FENCE();
      if (GM_B0) asm volatile("s_barrier" ::: "memory");
      asm volatile("s_waitcnt vmcnt(0)" : "+v"(xa[0]), "+v"(xa[1]), "+v"(xa[2]), "+v"(xa[3]), "+v"(xa[4]) :: "memory");
      if (g >= 2) xa[4] = v4u{0u, 0u, 0u, 0u};
      asm volatile("" : "+s"(e1));
      e1 += 1;
      const u64 tag = e1 & ((1ull << 40) - 1);
      if (lane == 0) *(LAS u64*)(L + L_MISC + 8) = e1;
      const int q4 = (lane >> 2) & 3, p4 = lane & 3, sw = 2 * (g & 1) + (q4 >> 1);
      const int rb = (4 * (g & 1) + q4) * 128 + 8 * p4;
      f32x4 qc[4];
#pragma unroll
      for (int cb = 0; cb < 4; ++cb) {
        qc[cb] = f32x4{0.f, 0.f, 0.f, 0.f};
        LAS unsigned char* tb = L + L_WO + rb + ((cb ^ sw) * 32);
#pragma unroll
        for (int ks = 0; ks < 5; ++ks) {
          LAS unsigned char* t2 = tb + ks * 4096 + (ks < 4 ? (g >> 1) * 2048 : 0);
          const v2u l2 = __builtin_bit_cast(v2u, __builtin_amdgcn_ds_read_tr16_b64_v4i16((LAS s16x4*)t2));
          const v2u h2 = __builtin_bit_cast(v2u, __builtin_amdgcn_ds_read_tr16_b64_v4i16((LAS s16x4*)(t2 + 1024)));
          qc[cb] = mfma(xa[ks], v4u{l2[0], l2[1], h2[0], h2[1]}, qc[cb]);
        }
      }
      GAS u64* qx = (GAS u64*)(a.ws + W_QX);
#pragma unroll
      for (int pr = 0; pr < 2; ++pr)
#pragma unroll
        for (int r = 0; r < 4; ++r) {
          const float val = g < 2 ? qc[2 * pr][r] : qc[2 * pr + 1][r];
          const u64 gr = (tag << 24) | (u64)((__builtin_bit_cast(unsigned, val) + 0x80u) >> 8);
          __hip_atomic_store(qx + ((size_t)(h * NB + 4 * (g & 1) + r) * 4 + kq) * 128 + 64 * ch + 16 * (2 * pr + (g >> 1)) + c, gr, __ATOMIC_RELAXED, __HIP_MEMORY_SCOPE_AGENT);
        }
      GAS u64* ib = qx + (size_t)(h * NB + b) * 512 + lane;
      u64 gv[8];
      gather8(ib, tag, gv);
      const float q0 = (GVAL(gv[0]) + GVAL(gv[2])) + (GVAL(gv[4]) + GVAL(gv[6]));
      const float q1 = (GVAL(gv[1]) + GVAL(gv[3])) + (GVAL(gv[5]) + GVAL(gv[7]));
      ((LAS u16*)(L + L_Q))[lane] = __builtin_bit_cast(u16, (__bf16)q0);
      ((LAS u16*)(L + L_Q))[64 + lane] = __builtin_bit_cast(u16, (__bf16)q1);
    }
    WG_BARRIER();
    const GAS unsigned char* wob = (const GAS unsigned char*)(a.wo + (size_t)h * DH * DM);
    int so[9];
#pragma unroll
    for (int j = 0; j < 9; ++j) {
      const int sl = 64 * j + lane, row = sl / 72, ch = sl % 72;
      so[j] = row * 1152 + ((((ch >> 1) ^ ((row >> 1) & 3)) << 1) | (ch & 1)) * 16;
    }
#pragma unroll 1
    for (int gr = 0; gr < 16; ++gr) {
#pragma unroll
      for (int j = 0; j < 9; ++j)
      { glds16(wob + gr * 9216 + so[j], (unsigned)(size_t)(L + L_WO) + gr * 9216 + j * 1024); AUX_THROTTLE(); }
    }
    asm volatile("s_waitcnt vmcnt(0)" ::: "memory");
  }
  STAMP(2);
  WG_BARRIER();
  STAMP(3);
  if (tid < DH) {
    const LAS float* ml = (const LAS float*)(L + L_ML);
    const LAS float* part = (const LAS float*)(L + L_PART);
    float mx = ml[0];
#pragma unroll
    for (int i = 1; i < NWS; ++i) mx = fmaxf(mx, ml[2 * i]);
    float den = 0.f, num = 0.f;
#pragma unroll
    for (int i = 0; i < NWS; ++i) {
      const float f = __builtin_amdgcn_exp2f(ml[2 * i] - mx);
      den = fmaf(f, ml[2 * i + 1], den); num = fmaf(f, part[i * DH + tid], num);
    }
    const int pos = (tid & 96) + 8 * ((tid >> 2) & 3) + 4 * ((tid >> 4) & 1) + (tid & 3);
    ((LAS u16*)(L + L_OBF))[pos] = __builtin_bit_cast(u16, (__bf16)(num / den));
  }
  WG_BARRIER();
  const float gate = *(const LAS float*)(L + L_MISC);
  const float omg = rbf(1.f - gate);
  const u64 ep1 = *(const LAS u64*)(L + L_MISC + 8);
  const u64 tag = ep1 & ((1ull << 40) - 1);
  {
    const int q4 = (lane >> 2) & 3, p4 = lane & 3;
    const int base = (4 * g + q4) * 1152 + ((w ^ ((2 * g + (q4 >> 1)) & 3)) * 32) + 8 * p4;
    f32x4 oc[3];
#pragma unroll
    for (int j = 0; j < 3; ++j) oc[j] = f32x4{0.f, 0.f, 0.f, 0.f};
#pragma unroll
    for (int ks = 0; ks < 4; ++ks) {
      const v4u af = *(const LAS v4u*)(L + L_OBF + 64 * ks + 16 * g);
#pragma unroll
      for (int j = 0; j < 3; ++j) {
        const s16x4 lo = __builtin_amdgcn_ds_read_tr16_b64_v4i16((LAS s16x4*)(L + L_WO + base + ks * 32 * 1152 + j * 384));
        const s16x4 hi = __builtin_amdgcn_ds_read_tr16_b64_v4i16((LAS s16x4*)(L + L_WO + base + (ks * 32 + 16) * 1152 + j * 384));
        const v2u l2 = __builtin_bit_cast(v2u, lo), h2 = __builtin_bit_cast(v2u, hi);
        oc[j] = mfma(af, v4u{l2[0], l2[1], h2[0], h2[1]}, oc[j]);
      }
    }
    if (lane < 48) {
      const int cb = w + 12 * g, n = 16 * cb + c;
      const float av = g == 0 ? oc[0][0] : (g == 1 ? oc[1][0] : oc[2][0]);
      const LAS float* mp = (const LAS float*)(L + L_MLP);
      const float tot = gate * av + omg * ((mp[n] + mp[DM + n]) + mp[2 * DM + n]);
      const u64 gr = (tag << 24) | (u64)((__builtin_bit_cast(unsigned, tot) + 0x80u) >> 8);
      __hip_atomic_store((GAS u64*)(a.ws + W_INBOX) + ((size_t)(b * 36 + cb) * 32 + h) * 16 + c, gr, __ATOMIC_RELAXED, __HIP_MEMORY_SCOPE_AGENT);
    }
  }
  STAMP(4);
  if (w == 0 || (w == 1 && h < 4)) {
    const int cb = h + 32 * w;
    const float b2v = bf2f(((const GAS u16*)a.b2)[16 * cb + c]);
    GAS u64* ib = (GAS u64*)(a.ws + W_INBOX) + (size_t)(b * 36 + cb) * 32 * 16 + lane;
    u64 gv[8];
    gather8(ib, tag, gv);
    float sum = 0.f;
#pragma unroll
    for (int i = 0; i < 8; ++i) sum += __builtin_bit_cast(float, (unsigned)gv[i] << 8);
    sum = xrow_sum(sum);
    if (lane < 16) ((GAS u16*)a.out)[b * DM + 16 * cb + c] = __builtin_bit_cast(u16, (__bf16)(sum + omg * b2v));
    if (w == 0 && bid == 0 && lane == 0)
      __hip_atomic_store((GAS u64*)(a.ws + W_EPOCH), ep1, __ATOMIC_RELAXED, __HIP_MEMORY_SCOPE_AGENT);
    STAMP(5);
  }
}

}

extern "C" void kernel_launch(void* const* d_in, const int* in_sizes, int n_in, void* d_out, int out_size, void* d_ws,
                              size_t ws_size, hipStream_t stream) {
  (void)in_sizes; (void)n_in; (void)out_size; (void)ws_size;
  gm::Args a;
  a.x = (const gm::u16*)d_in[0]; a.k = (const gm::u16*)d_in[1]; a.v = (const gm::u16*)d_in[2];
  a.wq = (const gm::u16*)d_in[3]; a.wo = (const gm::u16*)d_in[4];
  a.w1 = (const gm::u16*)d_in[6]; a.b1 = (const gm::u16*)d_in[7]; a.w2 = (const gm::u16*)d_in[8]; a.b2 = (const gm::u16*)d_in[9];
  a.gw = (const gm::u16*)d_in[10];
  a.out = (gm::u16*)d_out; a.ws = (unsigned char*)d_ws;
  (void)hipFuncSetAttribute((const void*)gm::fused_kernel, hipFuncAttributeMaxDynamicSharedMemorySize, gm::L_BYTES);
  hipLaunchKernelGGL(gm::fused_kernel, dim3(gm::NB * gm::NH), dim3(gm::NTHR), gm::L_BYTES, stream, a);
}
```

```cpp
#include <hip/hip_runtime.h>
#include <hip/hip_bf16.h>
#include <stdint.h>

namespace gm {

constexpr int NB = 8, NH = 32, SK = 4096, DH = 128, DM = 576, FF = 4608;
constexpr int FS = FF / NH;
constexpr int NWS = 8, NWA = 4, NTHR = (NWS + NWA) * 64;
constexpr int TK = 16;
constexpr int NT = SK / TK / NWS;
#define GM_DEPTH 3
constexpr int DEPTH = GM_DEPTH;
#define GM_WNT 1
#define GM_PRIO 1
#define GM_LATE 1
#define GM_B0 1
#define GM_AW 2
#define STR2(x) #x
#define STR(x) STR2(x)
#define GM_SW 2
#define GM_SW0 GM_SW
#define KV_THROTTLE() asm volatile("s_waitcnt vmcnt(" STR(GM_SW) ")" ::: "memory")
#define KV_THROTTLE0() asm volatile("s_waitcnt vmcnt(" STR(GM_SW0) ")" ::: "memory")
#define GM_ROT 0
#define GM_R1 3
#define AUX_THROTTLE() asm volatile("s_waitcnt vmcnt(" STR(GM_AW) ")" ::: "memory")

constexpr int L_WO = 0;
constexpr int L_PART = 128 * 1152;
constexpr int L_MLP = L_PART + 4096;
constexpr int L_Y = L_MLP + 3 * 2304;
constexpr int L_HID = L_Y + 3 * 1152;
constexpr int L_ML = L_HID + 3 * 192;
constexpr int L_OBF = L_ML + 64;
constexpr int L_Q = L_OBF + 256;
constexpr int L_MISC = L_Q + 256;
constexpr int L_BYTES = L_MISC + 16;
static_assert(L_BYTES <= 163840, "LDS");

constexpr size_t W_EPOCH = 0;
constexpr size_t W_QX = 4096;
constexpr size_t W_INBOX = W_QX + (size_t)NH * NB * 4 * 128 * 8;
constexpr size_t W_BYTES = W_INBOX + (size_t)NB * 36 * 32 * 16 * 8;

#define GAS __attribute__((address_space(1)))
#define LAS __attribute__((address_space(3)))
typedef unsigned v4u __attribute__((ext_vector_type(4)));
typedef unsigned v2u __attribute__((ext_vector_type(2)));
typedef float f32x4 __attribute__((ext_vector_type(4)));
typedef short s16x4 __attribute__((ext_vector_type(4)));
typedef __bf16 bf16x2 __attribute__((ext_vector_type(2)));
typedef __bf16 bf16x8 __attribute__((ext_vector_type(8)));
typedef unsigned long long u64;
typedef unsigned short u16;

struct Args {
  const u16* x; const u16* k; const u16* v; const u16* wq; const u16* wo;
  const u16* w1; const u16* b1; const u16* w2; const u16* b2; const u16* gw;
  u16* out; unsigned char* ws;
};

__device__ __forceinline__ v4u ld_nt(const GAS v4u* p) { return __builtin_nontemporal_load(p); }
__device__ __forceinline__ v4u ld_w(const GAS v4u* p) {
  return __builtin_nontemporal_load(p);
}
__device__ __forceinline__ void glds16(const GAS unsigned char* g, unsigned lds_base) {
  unsigned keep;
  asm volatile("s_mov_b32 %0, m0\n\ts_mov_b32 m0, %2\n\ts_nop 0\n\tglobal_load_lds_dwordx4 %1, off nt\n\ts_mov_b32 m0, %0" : "=&s"(keep) : "v"(g), "s"(lds_base) : "memory");
}
__device__ __forceinline__ float bf2f(unsigned h) { return __builtin_bit_cast(float, h << 16); }
__device__ __forceinline__ float bflo(unsigned u) { return __builtin_bit_cast(float, u << 16); }
__device__ __forceinline__ float bfhi(unsigned u) { return __builtin_bit_cast(float, u & 0xffff0000u); }
__device__ __forceinline__ unsigned pk_bf16(float lo, float hi) {
  bf16x2 p = {(__bf16)lo, (__bf16)hi};
  return __builtin_bit_cast(unsigned, p);
}
__device__ __forceinline__ float rbf(float f) { return (float)(__bf16)f; }
__device__ __forceinline__ float dot2(unsigned a, unsigned b, float c) {
  return __builtin_amdgcn_fdot2_f32_bf16(__builtin_bit_cast(bf16x2, a), __builtin_bit_cast(bf16x2, b), c, false);
}
__device__ __forceinline__ f32x4 mfma(v4u a, v4u b, f32x4 c) {
  return __builtin_amdgcn_mfma_f32_16x16x32_bf16(__builtin_bit_cast(bf16x8, a), __builtin_bit_cast(bf16x8, b), c, 0, 0, 0);
}
template <int CTRL> __device__ __forceinline__ float dppf(float x) {
  return __builtin_bit_cast(float, __builtin_amdgcn_mov_dpp(__builtin_bit_cast(int, x), CTRL, 0xf, 0xf, true));
}
template <int CTRL> __device__ __forceinline__ unsigned dppu(unsigned x) {
  return (unsigned)__builtin_amdgcn_mov_dpp((int)x, CTRL, 0xf, 0xf, true);
}
constexpr int XOR1 = 0xB1, XOR2 = 0x4E, XOR7 = 0x141, XOR8 = 0x128, BCAST = 0x150;
__device__ __forceinline__ float xrow_sum(float x) {
  auto s = __builtin_amdgcn_permlane16_swap(__float_as_uint(x), __float_as_uint(x), false, false);
  x = __uint_as_float(s[0]) + __uint_as_float(s[1]);
  auto t = __builtin_amdgcn_permlane32_swap(__float_as_uint(x), __float_as_uint(x), false, false);
  return __uint_as_float(t[0]) + __uint_as_float(t[1]);
}
__device__ __forceinline__ float xrow_max(float x) {
  auto s = __builtin_amdgcn_permlane16_swap(__float_as_uint(x), __float_as_uint(x), false, false);
  x = fmaxf(__uint_as_float(s[0]), __uint_as_float(s[1]));
  auto t = __builtin_amdgcn_permlane32_swap(__float_as_uint(x), __float_as_uint(x), false, false);
  return fmaxf(__uint_as_float(t[0]), __uint_as_float(t[1]));
}
__device__ __forceinline__ float fold_halves(float x) {
  const float t = dppf<XOR8>(x);
  auto p = __builtin_amdgcn_permlane32_swap(__float_as_uint(t), __float_as_uint(t), false, false);
  return x + __uint_as_float(p[1]);
}
__device__ __forceinline__ float wave_sum(float x) {
  x += dppf<XOR1>(x); x += dppf<XOR2>(x); x += dppf<XOR7>(x); x += dppf<XOR8>(x);
  return xrow_sum(x);
}
__device__ __forceinline__ float wave_max(float x) {
  x = fmaxf(x, dppf<XOR1>(x)); x = fmaxf(x, dppf<XOR2>(x)); x = fmaxf(x, dppf<XOR7>(x)); x = fmaxf(x, dppf<XOR8>(x));
  return xrow_max(x);
}
#define WG_BARRIER() asm volatile("s_waitcnt lgkmcnt(0)\n\ts_barrier" ::: "memory")
#define SCHED_FENCE() __builtin_amdgcn_sched_barrier(0)

#define STAMP(i) do { } while (0)

constexpr float SCL = 0.088388347648318440f * 1.4426950408889634f;

#define GVAL(x) __builtin_bit_cast(float, (unsigned)(x) << 8)
__device__ __forceinline__ void gather8(GAS u64* ib, u64 tag, u64 (&gv)[8]) {
#pragma unroll
  for (int i = 0; i < 8; ++i) gv[i] = __hip_atomic_load(ib + 64 * i, __ATOMIC_RELAXED, __HIP_MEMORY_SCOPE_AGENT);
  for (unsigned spin = 0; spin < (1u << 22); ++spin) {
    bool all = true;
#pragma unroll
    for (int i = 0; i < 8; ++i) all = all && (gv[i] >> 24) == tag;
    if (__builtin_amdgcn_ballot_w64(!all) == 0) break;
    __builtin_amdgcn_s_sleep(1);
#pragma unroll
    for (int i = 0; i < 8; ++i)
      if ((gv[i] >> 24) != tag) gv[i] = __hip_atomic_load(ib + 64 * i, __ATOMIC_RELAXED, __HIP_MEMORY_SCOPE_AGENT);
  }
}

struct Soft { float m, mc, l; f32x4 acc; };

#define GM_KDEF 99
#define GM_VDEF 99
template <bool NT> __device__ __forceinline__ v4u ld_p(const GAS v4u* p) { if (NT) return __builtin_nontemporal_load(p); else return *p; }
template <bool ISSUE, bool KNT, bool VNT>
__device__ __forceinline__ void tile_step(v4u (&kr)[4], v4u (&vr)[4], const GAS v4u* kn, const GAS v4u* vn, const v4u q,
                                          const unsigned (&mask)[4], bool hi8, bool hi4, Soft& s) {
  float x[4];
#pragma unroll
  for (int t = 0; t < 4; ++t) {
    float d = dot2(kr[t][0], q[0], 0.f);
    d = dot2(kr[t][1], q[1], d); d = dot2(kr[t][2], q[2], d); d = dot2(kr[t][3], q[3], d);
    x[t] = d;
    if (ISSUE) { asm volatile("" : "+v"(x[t])); KV_THROTTLE(); kr[t] = ld_p<KNT>(kn + t * 64); SCHED_FENCE(); }
  }
  const float a0 = x[0] + dppf<XOR8>(x[0]), a2 = x[2] + dppf<XOR8>(x[2]);
  const float a1 = x[1] + dppf<XOR8>(x[1]), a3 = x[3] + dppf<XOR8>(x[3]);
  const float y0 = hi8 ? a2 : a0, y1 = hi8 ? a3 : a1;
  const float b0 = y0 + dppf<XOR7>(y0), b1 = y1 + dppf<XOR7>(y1);
  float z = hi4 ? b1 : b0;
  z += dppf<XOR2>(z); z += dppf<XOR1>(z);
  if (__builtin_amdgcn_ballot_w64(z > s.m) != 0) {
    const float mn = fmaxf(s.m, wave_max(z));
    const float mcn = mn * SCL;
    const float alpha = __builtin_amdgcn_exp2f(s.mc - mcn);
    s.m = mn; s.mc = mcn; s.l *= alpha; s.acc *= alpha;
  }
  const float p = __builtin_amdgcn_exp2f(fmaf(z, SCL, -s.mc));
  s.l += p;
  const unsigned pk = pk_bf16(p, p);
#define PV_STEP(t) { const v4u bf = {dppu<BCAST + 4 * t>(pk) & mask[0], dppu<BCAST + 4 * t>(pk) & mask[1], dppu<BCAST + 4 * t>(pk) & mask[2], dppu<BCAST + 4 * t>(pk) & mask[3]}; \
    s.acc = mfma(vr[t], bf, s.acc); if (ISSUE) { SCHED_FENCE(); KV_THROTTLE(); vr[t] = ld_p<VNT>(vn + t * 64); SCHED_FENCE(); } }
  PV_STEP(0) PV_STEP(1) PV_STEP(2) PV_STEP(3)
#undef PV_STEP
}

__global__ void __launch_bounds__(NTHR, 3) fused_kernel(Args a) {
  extern __shared__ __attribute__((aligned(16))) unsigned char smem[];
  LAS unsigned char* L = (LAS unsigned char*)smem;
  const int tid = threadIdx.x, lane = tid & 63, w = __builtin_amdgcn_readfirstlane(tid >> 6);
  const int bid = blockIdx.x;
  const int h = (bid & 7) * 4 + ((bid >> 3) & 3), b = bid >> 5;
  const int g = lane >> 4, c = lane & 15;

  if (w >= NWA) {
    if (b >= GM_KDEF) {
    const int ws_ = w - NWA;
    const size_t kvoff = ((size_t)(b * NH + h) * SK + ws_ * TK) * DH;
    const GAS v4u* kp = (const GAS v4u*)(a.k + kvoff) + lane;
    const GAS v4u* vp = (const GAS v4u*)(a.v + kvoff) + lane;
    constexpr int TSTR = NWS * TK * DH / 8;
    v4u kr[DEPTH][4], vr[DEPTH][4];
    int rot = GM_ROT == 0 ? 0 : GM_ROT == 1 ? (int)((bid * 2654435761u) >> 27) : GM_ROT == 2 ? (int)(((bid * 8 + ws_) * 2654435761u) >> 27) : GM_ROT == 3 ? (bid >> 5) * 4 : GM_ROT == 4 ? ws_ * 4 : (bid >> 3);
#define TOFF(i) ((((i) + rot) & (NT - 1)) * TSTR)
    if (GM_B0) WG_BARRIER();
#pragma unroll
    for (int d = 0; d < DEPTH; ++d) {
#pragma unroll
      for (int t = 0; t < 4; ++t) { KV_THROTTLE0(); kr[d][t] = ld_p<false>(kp + TOFF(d) + t * 64); SCHED_FENCE(); }
#pragma unroll
      for (int t = 0; t < 4; ++t) { KV_THROTTLE0(); vr[d][t] = ld_p<true>(vp + TOFF(d) + t * 64); SCHED_FENCE(); }
    }
    unsigned mask[4];
#pragma unroll
    for (int r = 0; r < 4; ++r) mask[r] = (c >> 1) == r ? ((c & 1) ? 0xffff0000u : 0x0000ffffu) : 0u;
    LAS float* part = (LAS float*)(L + L_PART);
    WG_BARRIER();
    const v4u q = *(const LAS v4u*)(L + L_Q + 16 * c);
    Soft s; s.m = -INFINITY; s.mc = -INFINITY; s.l = 0.f; s.acc = f32x4{0.f, 0.f, 0.f, 0.f};
    const bool hi8 = (c & 8) != 0, hi4 = (c & 4) != 0;
    constexpr int REM = NT % DEPTH;
#pragma unroll 1
    for (int it = 0; it < NT / DEPTH - 1; ++it) {
      rot += DEPTH;
#pragma unroll
      for (int d = 0; d < DEPTH; ++d) tile_step<true, false, true>(kr[d], vr[d], kp + TOFF(d), vp + TOFF(d), q, mask, hi8, hi4, s);
    }
    rot += DEPTH;
#pragma unroll
    for (int d = 0; d < DEPTH; ++d) {
      if (d < REM) tile_step<true, false, true>(kr[d], vr[d], kp + TOFF(d), vp + TOFF(d), q, mask, hi8, hi4, s);
      else tile_step<false, false, true>(kr[d], vr[d], kp, vp, q, mask, hi8, hi4, s);
    }
#pragma unroll
    for (int d = 0; d < REM; ++d) tile_step<false, false, true>(kr[d], vr[d], kp, vp, q, mask, hi8, hi4, s);
    const float lsum = wave_sum(s.l) * 0.25f;
    if (c < 8) {
#pragma unroll
      for (int r = 0; r < 4; ++r) part[ws_ * DH + 8 * (4 * g + r) + c] = s.acc[r];
    }
    if (lane == 0) { ((LAS float*)(L + L_ML))[2 * ws_] = s.mc; ((LAS float*)(L + L_ML))[2 * ws_ + 1] = lsum; }
    } else if (b >= GM_VDEF) {
    const int ws_ = w - NWA;
    const size_t kvoff = ((size_t)(b * NH + h) * SK + ws_ * TK) * DH;
    const GAS v4u* kp = (const GAS v4u*)(a.k + kvoff) + lane;
    const GAS v4u* vp = (const GAS v4u*)(a.v + kvoff) + lane;
    constexpr int TSTR = NWS * TK * DH / 8;
    v4u kr[DEPTH][4], vr[DEPTH][4];
    int rot = GM_ROT == 0 ? 0 : GM_ROT == 1 ? (int)((bid * 2654435761u) >> 27) : GM_ROT == 2 ? (int)(((bid * 8 + ws_) * 2654435761u) >> 27) : GM_ROT == 3 ? (bid >> 5) * 4 : GM_ROT == 4 ? ws_ * 4 : (bid >> 3);
#define TOFF(i) ((((i) + rot) & (NT - 1)) * TSTR)
    if (GM_B0) WG_BARRIER();
#pragma unroll
    for (int d = 0; d < DEPTH; ++d) {
#pragma unroll
      for (int t = 0; t < 4; ++t) { KV_THROTTLE0(); kr[d][t] = ld_p<true>(kp + TOFF(d) + t * 64); SCHED_FENCE(); }
#pragma unroll
      for (int t = 0; t < 4; ++t) { KV_THROTTLE0(); vr[d][t] = ld_p<false>(vp + TOFF(d) + t * 64); SCHED_FENCE(); }
    }
    unsigned mask[4];
#pragma unroll
    for (int r = 0; r < 4; ++r) mask[r] = (c >> 1) == r ? ((c & 1) ? 0xffff0000u : 0x0000ffffu) : 0u;
    LAS float* part = (LAS float*)(L + L_PART);
    WG_BARRIER();
    const v4u q = *(const LAS v4u*)(L + L_Q + 16 * c);
    Soft s; s.m = -INFINITY; s.mc = -INFINITY; s.l = 0.f; s.acc = f32x4{0.f, 0.f, 0.f, 0.f};
    const bool hi8 = (c & 8) != 0, hi4 = (c & 4) != 0;
    constexpr int REM = NT % DEPTH;
#pragma unroll 1
    for (int it = 0; it < NT / DEPTH - 1; ++it) {
      rot += DEPTH;
#pragma unroll
      for (int d = 0; d < DEPTH; ++d) tile_step<true, true, false>(kr[d], vr[d], kp + TOFF(d), vp + TOFF(d), q, mask, hi8, hi4, s);
    }
    rot += DEPTH;
#pragma unroll
    for (int d = 0; d < DEPTH; ++d) {
      if (d < REM) tile_step<true, true, false>(kr[d], vr[d], kp + TOFF(d), vp + TOFF(d), q, mask, hi8, hi4, s);
      else tile_step<false, true, false>(kr[d], vr[d], kp, vp, q, mask, hi8, hi4, s);
    }
#pragma unroll
    for (int d = 0; d < REM; ++d) tile_step<false, true, false>(kr[d], vr[d], kp, vp, q, mask, hi8, hi4, s);
    const float lsum = wave_sum(s.l) * 0.25f;
    if (c < 8) {
#pragma unroll
      for (int r = 0; r < 4; ++r) part[ws_ * DH + 8 * (4 * g + r) + c] = s.acc[r];
    }
    if (lane == 0) { ((LAS float*)(L + L_ML))[2 * ws_] = s.mc; ((LAS float*)(L + L_ML))[2 * ws_ + 1] = lsum; }
    } else {
    const int ws_ = w - NWA;
    const size_t kvoff = ((size_t)(b * NH + h) * SK + ws_ * TK) * DH;
    const GAS v4u* kp = (const GAS v4u*)(a.k + kvoff) + lane;
    const GAS v4u* vp = (const GAS v4u*)(a.v + kvoff) + lane;
    constexpr int TSTR = NWS * TK * DH / 8;
    v4u kr[DEPTH][4], vr[DEPTH][4];
    int rot = GM_ROT == 0 ? 0 : GM_ROT == 1 ? (int)((bid * 2654435761u) >> 27) : GM_ROT == 2 ? (int)(((bid * 8 + ws_) * 2654435761u) >> 27) : GM_ROT == 3 ? (bid >> 5) * 4 : GM_ROT == 4 ? ws_ * 4 : (bid >> 3);
#define TOFF(i) ((((i) + rot) & (NT - 1)) * TSTR)
    if (GM_B0) WG_BARRIER();
#pragma unroll
    for (int d = 0; d < DEPTH; ++d) {
#pragma unroll
      for (int t = 0; t < 4; ++t) { KV_THROTTLE0(); kr[d][t] = ld_p<true>(kp + TOFF(d) + t * 64); SCHED_FENCE(); }
#pragma unroll
      for (int t = 0; t < 4; ++t) { KV_THROTTLE0(); vr[d][t] = ld_p<true>(vp + TOFF(d) + t * 64); SCHED_FENCE(); }
    }
    unsigned mask[4];
#pragma unroll
    for (int r = 0; r < 4; ++r) mask[r] = (c >> 1) == r ? ((c & 1) ? 0xffff0000u : 0x0000ffffu) : 0u;
    LAS float* part = (LAS float*)(L + L_PART);
    WG_BARRIER();
    const v4u q = *(const LAS v4u*)(L + L_Q + 16 * c);
    Soft s; s.m = -INFINITY; s.mc = -INFINITY; s.l = 0.f; s.acc = f32x4{0.f, 0.f, 0.f, 0.f};
    const bool hi8 = (c & 8) != 0, hi4 = (c & 4) != 0;
    constexpr int REM = NT % DEPTH;
#pragma unroll 1
    for (int it = 0; it < NT / DEPTH - 1; ++it) {
      rot += DEPTH;
#pragma unroll
      for (int d = 0; d < DEPTH; ++d) tile_step<true, true, true>(kr[d], vr[d], kp + TOFF(d), vp + TOFF(d), q, mask, hi8, hi4, s);
    }
    rot += DEPTH;
#pragma unroll
    for (int d = 0; d < DEPTH; ++d) {
      if (d < REM) tile_step<true, true, true>(kr[d], vr[d], kp + TOFF(d), vp + TOFF(d), q, mask, hi8, hi4, s);
      else tile_step<false, true, true>(kr[d], vr[d], kp, vp, q, mask, hi8, hi4, s);
    }
#pragma unroll
    for (int d = 0; d < REM; ++d) tile_step<false, true, true>(kr[d], vr[d], kp, vp, q, mask, hi8, hi4, s);
    const float lsum = wave_sum(s.l) * 0.25f;
    if (c < 8) {
#pragma unroll
      for (int r = 0; r < 4; ++r) part[ws_ * DH + 8 * (4 * g + r) + c] = s.acc[r];
    }
    if (lane == 0) { ((LAS float*)(L + L_ML))[2 * ws_] = s.mc; ((LAS float*)(L + L_ML))[2 * ws_ + 1] = lsum; }
    }
  } else if (w > 0) {
    const int m = w - 1;
    if (GM_PRIO) __builtin_amdgcn_s_setprio(3);
    if (GM_B0) WG_BARRIER();
    WG_BARRIER();
    const GAS u16* xp = (const GAS u16*)(a.x + b * DM);
    float xv[9];
#pragma unroll
    for (int i = 0; i < 9; ++i) xv[i] = bf2f(xp[lane + 64 * i]);
    const int col0 = 64 * ((9 * h) / 4 + m), sl = 4 * (c >> 3) + g;
    const GAS unsigned char* w1p = (const GAS unsigned char*)a.w1 + (size_t)(72 * sl) * (FF * 2) + col0 * 2 + (c & 7) * 16;
    constexpr int R1 = GM_R1;
    v4u r1[R1];
#pragma unroll
    for (int i = 0; i < R1; ++i) { r1[i] = ld_w((const GAS v4u*)(w1p + i * (FF * 2))); SCHED_FENCE(); }
    float b1v[4];
#pragma unroll
    for (int r = 0; r < 4; ++r) b1v[r] = bf2f(((const GAS u16*)a.b1)[col0 + 8 * (4 * (g & 1) + r) + (c & 7)]);
    if (m == 0) {
      const GAS u16* gp = (const GAS u16*)a.gw;
      float gs = 0.f;
#pragma unroll
      for (int i = 0; i < 9; ++i) gs += rbf(xv[i] * bf2f(gp[lane + 64 * i]));
      const float logit = rbf(wave_sum(gs));
      const float gate = rbf(1.f / (1.f + __expf(-logit)));
      if (lane == 0) *(LAS float*)(L + L_MISC) = gate;
    }
    float sum = 0.f;
#pragma unroll
    for (int i = 0; i < 9; ++i) sum += xv[i];
    const float mean = wave_sum(sum) * (1.f / DM);
    float sq = 0.f;
#pragma unroll
    for (int i = 0; i < 9; ++i) sq += (xv[i] - mean) * (xv[i] - mean);
    const float var = rbf(wave_sum(sq) * (1.f / DM));
    const float mb = rbf(mean), sd = rbf(sqrtf(rbf(var + 1e-5f)));
    LAS u16* yl = (LAS u16*)(L + L_Y + m * 1152);
#pragma unroll
    for (int i = 0; i < 9; ++i) yl[lane + 64 * i] = __builtin_bit_cast(u16, (__bf16)(rbf(xv[i] - mb) / sd));
    asm volatile("s_waitcnt lgkmcnt(0)" ::: "memory");
    unsigned mask[4];
#pragma unroll
    for (int r = 0; r < 4; ++r) mask[r] = ((c & 7) >> 1) == r ? ((c & 1) ? 0xffff0000u : 0x0000ffffu) : 0u;
    f32x4 h1 = {0.f, 0.f, 0.f, 0.f};
    static_assert(24 % R1 == 0, "ring");
#pragma unroll 1
    for (int o = 0; o < 72; o += 24) {
      v4u yy[3];
#pragma unroll
      for (int i = 0; i < 3; ++i) yy[i] = *(const LAS v4u*)(yl + 72 * sl + o + 8 * i);
#pragma unroll
      for (int i = 0; i < 24; ++i) {
        const unsigned u = yy[i >> 3][(i >> 1) & 3];
        const unsigned dup = __builtin_amdgcn_perm(u, u, (i & 1) ? 0x03020302u : 0x01000100u);
        h1 = mfma(r1[i % R1], v4u{dup & mask[0], dup & mask[1], dup & mask[2], dup & mask[3]}, h1);
        SCHED_FENCE();
        const int nx = o + i + R1 < 72 ? o + i + R1 : 71;
        r1[i % R1] = ld_w((const GAS v4u*)(w1p + (size_t)nx * (FF * 2)));
        SCHED_FENCE();
      }
    }
    const int lo = col0 > FS * h ? col0 : FS * h, hi = col0 + 64 < FS * h + FS ? col0 + 64 : FS * h + FS;
    const int nrg = (hi - lo) >> 3;
    const GAS unsigned char* w2p = (const GAS unsigned char*)a.w2 + (size_t)(lo + sl) * (DM * 2) + (c & 7) * 16;
    v4u r2[3];
#pragma unroll
    for (int i = 0; i < 3; ++i) { r2[i] = ld_w((const GAS v4u*)(w2p + i * 128)); SCHED_FENCE(); }
    LAS u16* hid = (LAS u16*)(L + L_HID + m * 128);
#pragma unroll
    for (int r = 0; r < 4; ++r) {
      const float pre = rbf(rbf(fold_halves(h1[r])) + b1v[r]);
      const float inner = 0.7978845608028654f * (pre + 0.044715f * pre * pre * pre);
      const float th = 1.f - 2.f / (1.f + __expf(2.f * inner));
      if (lane < 32 && c < 8) hid[8 * (4 * g + r) + c] = __builtin_bit_cast(u16, (__bf16)(0.5f * pre * (1.f + th)));
    }
    asm volatile("s_waitcnt lgkmcnt(0)" ::: "memory");
    f32x4 o2[9];
#pragma unroll
    for (int j = 0; j < 9; ++j) o2[j] = f32x4{0.f, 0.f, 0.f, 0.f};
#pragma unroll 1
    for (int rg = 0; rg < nrg; ++rg) {
      const unsigned u = hid[lo - col0 + 8 * rg + sl];
      const unsigned dup = u | (u << 16);
      const v4u bf = {dup & mask[0], dup & mask[1], dup & mask[2], dup & mask[3]};
      const GAS unsigned char* cur = w2p + (size_t)rg * (8 * DM * 2);
      const GAS unsigned char* nxt = w2p + (size_t)(rg + 1 < nrg ? rg + 1 : rg) * (8 * DM * 2);
#pragma unroll
      for (int j = 0; j < 9; ++j) {
        o2[j] = mfma(r2[j % 3], bf, o2[j]);
        SCHED_FENCE();
        r2[j % 3] = ld_w((const GAS v4u*)(j < 6 ? cur + (j + 3) * 128 : nxt + (j - 6) * 128));
        SCHED_FENCE();
      }
    }
    LAS float* mp = (LAS float*)(L + L_MLP + m * 2304);
#pragma unroll
    for (int j = 0; j < 9; ++j)
#pragma unroll
      for (int r = 0; r < 4; ++r) {
        const float t = fold_halves(o2[j][r]);
        if (lane < 32 && c < 8) mp[64 * j + 8 * (4 * g + r) + c] = t;
      }
  } else {
    if (GM_PRIO) __builtin_amdgcn_s_setprio(3);
    {
      u64 e1 = *(const __attribute__((address_space(4))) u64*)(a.ws + W_EPOCH);
      const int ch = b & 1, kq = b >> 1;
      const GAS unsigned char* wqb = (const GAS unsigned char*)a.wq + (size_t)(144 * kq + 8 * (lane >> 5) + ((lane >> 3) & 3)) * (NH * DH * 2)
                                     + h * 256 + ch * 128 + (((((lane & 7) >> 1) ^ g) << 1) | (lane & 1)) * 16;
#pragma unroll
      for (int i = 0; i < 18; ++i) glds16(wqb + (size_t)(16 * (i >> 1) + 4 * (i & 1)) * (NH * DH * 2), (unsigned)(size_t)(L + L_WO) + i * 1024);
      const GAS unsigned char* xb = (const GAS unsigned char*)a.x + (c & 7) * (DM * 2) + 288 * kq;
      v4u xa[5];
#pragma unroll
      for (int ks = 0; ks < 4; ++ks) xa[ks] = *(const GAS v4u*)(xb + 64 * ks + 16 * g);
      xa[4] = *(const GAS v4u*)(xb + 256 + 16 * (g & 1));
      SCHED_FENCE();
      if (GM_B0) asm volatile("s_barrier" ::: "memory");
      asm volatile("s_waitcnt vmcnt(0)" : "+v"(xa[0]), "+v"(xa[1]), "+v"(xa[2]), "+v"(xa[3]), "+v"(xa[4]) :: "memory");
      if (g >= 2) xa[4] = v4u{0u, 0u, 0u, 0u};
      asm volatile("" : "+s"(e1));
      e1 += 1;
      const u64 tag = e1 & ((1ull << 40) - 1);
      if (lane == 0) *(LAS u64*)(L + L_MISC + 8) = e1;
      const int q4 = (lane >> 2) & 3, p4 = lane & 3, sw = 2 * (g & 1) + (q4 >> 1);
      const int rb = (4 * (g & 1) + q4) * 128 + 8 * p4;
      f32x4 qc[4];
#pragma unroll
      for (int cb = 0; cb < 4; ++cb) {
        qc[cb] = f32x4{0.f, 0.f, 0.f, 0.f};
        LAS unsigned char* tb = L + L_WO + rb + ((cb ^ sw) * 32);
#pragma unroll
        for (int ks = 0; ks < 5; ++ks) {
          LAS unsigned char* t2 = tb + ks * 4096 + (ks < 4 ? (g >> 1) * 2048 : 0);
          const v2u l2 = __builtin_bit_cast(v2u, __builtin_amdgcn_ds_read_tr16_b64_v4i16((LAS s16x4*)t2));
          const v2u h2 = __builtin_bit_cast(v2u, __builtin_amdgcn_ds_read_tr16_b64_v4i16((LAS s16x4*)(t2 + 1024)));
          qc[cb] = mfma(xa[ks], v4u{l2[0], l2[1], h2[0], h2[1]}, qc[cb]);
        }
      }
      GAS u64* qx = (GAS u64*)(a.ws + W_QX);
#pragma unroll
      for (int pr = 0; pr < 2; ++pr)
#pragma unroll
        for (int r = 0; r < 4; ++r) {
          const float val = g < 2 ? qc[2 * pr][r] : qc[2 * pr + 1][r];
          const u64 gr = (tag << 24) | (u64)((__builtin_bit_cast(unsigned, val) + 0x80u) >> 8);
          __hip_atomic_store(qx + ((size_t)(h * NB + 4 * (g & 1) + r) * 4 + kq) * 128 + 64 * ch + 16 * (2 * pr + (g >> 1)) + c, gr, __ATOMIC_RELAXED, __HIP_MEMORY_SCOPE_AGENT);
        }
      GAS u64* ib = qx + (size_t)(h * NB + b) * 512 + lane;
      u64 gv[8];
      gather8(ib, tag, gv);
      const float q0 = (GVAL(gv[0]) + GVAL(gv[2])) + (GVAL(gv[4]) + GVAL(gv[6]));
      const float q1 = (GVAL(gv[1]) + GVAL(gv[3])) + (GVAL(gv[5]) + GVAL(gv[7]));
      ((LAS u16*)(L + L_Q))[lane] = __builtin_bit_cast(u16, (__bf16)q0);
      ((LAS u16*)(L + L_Q))[64 + lane] = __builtin_bit_cast(u16, (__bf16)q1);
    }
    WG_BARRIER();
    const GAS unsigned char* wob = (const GAS unsigned char*)(a.wo + (size_t)h * DH * DM);
    int so[9];
#pragma unroll
    for (int j = 0; j < 9; ++j) {
      const int sl = 64 * j + lane, row = sl / 72, ch = sl % 72;
      so[j] = row * 1152 + ((((ch >> 1) ^ ((row >> 1) & 3)) << 1) | (ch & 1)) * 16;
    }
#pragma unroll 1
    for (int gr = 0; gr < 16; ++gr) {
#pragma unroll
      for (int j = 0; j < 9; ++j)
      { glds16(wob + gr * 9216 + so[j], (unsigned)(size_t)(L + L_WO) + gr * 9216 + j * 1024); AUX_THROTTLE(); }
    }
    asm volatile("s_waitcnt vmcnt(0)" ::: "memory");
  }
  STAMP(2);
  WG_BARRIER();
  STAMP(3);
  if (tid < DH) {
    const LAS float* ml = (const LAS float*)(L + L_ML);
    const LAS float* part = (const LAS float*)(L + L_PART);
    float mx = ml[0];
#pragma unroll
    for (int i = 1; i < NWS; ++i) mx = fmaxf(mx, ml[2 * i]);
    float den = 0.f, num = 0.f;
#pragma unroll
    for (int i = 0; i < NWS; ++i) {
      const float f = __builtin_amdgcn_exp2f(ml[2 * i] - mx);
      den = fmaf(f, ml[2 * i + 1], den); num = fmaf(f, part[i * DH + tid], num);
    }
    const int pos = (tid & 96) + 8 * ((tid >> 2) & 3) + 4 * ((tid >> 4) & 1) + (tid & 3);
    ((LAS u16*)(L + L_OBF))[pos] = __builtin_bit_cast(u16, (__bf16)(num / den));
  }
  WG_BARRIER();
  const float gate = *(const LAS float*)(L + L_MISC);
  const float omg = rbf(1.f - gate);
  const u64 ep1 = *(const LAS u64*)(L + L_MISC + 8);
  const u64 tag = ep1 & ((1ull << 40) - 1);
  {
    const int q4 = (lane >> 2) & 3, p4 = lane & 3;
    const int base = (4 * g + q4) * 1152 + ((w ^ ((2 * g + (q4 >> 1)) & 3)) * 32) + 8 * p4;
    f32x4 oc[3];
#pragma unroll
    for (int j = 0; j < 3; ++j) oc[j] = f32x4{0.f, 0.f, 0.f, 0.f};
#pragma unroll
    for (int ks = 0; ks < 4; ++ks) {
      const v4u af = *(const LAS v4u*)(L + L_OBF + 64 * ks + 16 * g);
#pragma unroll
      for (int j = 0; j < 3; ++j) {
        const s16x4 lo = __builtin_amdgcn_ds_read_tr16_b64_v4i16((LAS s16x4*)(L + L_WO + base + ks * 32 * 1152 + j * 384));
        const s16x4 hi = __builtin_amdgcn_ds_read_tr16_b64_v4i16((LAS s16x4*)(L + L_WO + base + (ks * 32 + 16) * 1152 + j * 384));
        const v2u l2 = __builtin_bit_cast(v2u, lo), h2 = __builtin_bit_cast(v2u, hi);
        oc[j] = mfma(af, v4u{l2[0], l2[1], h2[0], h2[1]}, oc[j]);
      }
    }
    if (lane < 48) {
      const int cb = w + 12 * g, n = 16 * cb + c;
      const float av = g == 0 ? oc[0][0] : (g == 1 ? oc[1][0] : oc[2][0]);
      const LAS float* mp = (const LAS float*)(L + L_MLP);
      const float tot = gate * av + omg * ((mp[n] + mp[DM + n]) + mp[2 * DM + n]);
      const u64 gr = (tag << 24) | (u64)((__builtin_bit_cast(unsigned, tot) + 0x80u) >> 8);
      __hip_atomic_store((GAS u64*)(a.ws + W_INBOX) + ((size_t)(b * 36 + cb) * 32 + h) * 16 + c, gr, __ATOMIC_RELAXED, __HIP_MEMORY_SCOPE_AGENT);
    }
  }
  STAMP(4);
  if (w == 0 || (w == 1 && h < 4)) {
    const int cb = h + 32 * w;
    const float b2v = bf2f(((const GAS u16*)a.b2)[16 * cb + c]);
    GAS u64* ib = (GAS u64*)(a.ws + W_INBOX) + (size_t)(b * 36 + cb) * 32 * 16 + lane;
    u64 gv[8];
    gather8(ib, tag, gv);
    float sum = 0.f;
#pragma unroll
    for (int i = 0; i < 8; ++i) sum += __builtin_bit_cast(float, (unsigned)gv[i] << 8);
    sum = xrow_sum(sum);
    if (lane < 16) ((GAS u16*)a.out)[b * DM + 16 * cb + c] = __builtin_bit_cast(u16, (__bf16)(sum + omg * b2v));
    if (w == 0 && bid == 0 && lane == 0)
      __hip_atomic_store((GAS u64*)(a.ws + W_EPOCH), ep1, __ATOMIC_RELAXED, __HIP_MEMORY_SCOPE_AGENT);
    STAMP(5);
  }
}

}

extern "C" void kernel_launch(void* const* d_in, const int* in_sizes, int n_in, void* d_out, int out_size, void* d_ws,
                              size_t ws_size, hipStream_t stream) {
  (void)in_sizes; (void)n_in; (void)out_size; (void)ws_size;
  gm::Args a;
  a.x = (const gm::u16*)d_in[0]; a.k = (const gm::u16*)d_in[1]; a.v = (const gm::u16*)d_in[2];
  a.wq = (const gm::u16*)d_in[3]; a.wo = (const gm::u16*)d_in[4];
  a.w1 = (const gm::u16*)d_in[6]; a.b1 = (const gm::u16*)d_in[7]; a.w2 = (const gm::u16*)d_in[8]; a.b2 = (const gm::u16*)d_in[9];
  a.gw = (const gm::u16*)d_in[10];
  a.out = (gm::u16*)d_out; a.ws = (unsigned char*)d_ws;
  (void)hipFuncSetAttribute((const void*)gm::fused_kernel, hipFuncAttributeMaxDynamicSharedMemorySize, gm::L_BYTES);
  hipLaunchKernelGGL(gm::fused_kernel, dim3(gm::NB * gm::NH), dim3(gm::NTHR), gm::L_BYTES, stream, a);
}
```

```cpp
#define DG_MODE 0
#include <hip/hip_runtime.h>
#include <hip/hip_bf16.h>
#include <stdint.h>

namespace gm {

constexpr int NB = 8, NH = 32, SK = 4096, DH = 128, DM = 576, FF = 4608;
constexpr int FS = FF / NH;
constexpr int NWS = 8, NWA = 4, NTHR = (NWS + NWA) * 64;
constexpr int TK = 16;
constexpr int NT = SK / TK / NWS;
#define GM_DEPTH 3
constexpr int DEPTH = GM_DEPTH;
#define GM_WNT 1
#define GM_PRIO 1
#define GM_LATE 1
#define GM_B0 1
#define GM_AW 2
#define STR2(x) #x
#define STR(x) STR2(x)
#define GM_SW 2
#define GM_SW0 GM_SW
#define KV_THROTTLE() asm volatile("s_waitcnt vmcnt(" STR(GM_SW) ")" ::: "memory")
#define KV_THROTTLE0() asm volatile("s_waitcnt vmcnt(" STR(GM_SW0) ")" ::: "memory")
#define GM_ROT 0
#define GM_R1 3
#define AUX_THROTTLE() asm volatile("s_waitcnt vmcnt(" STR(GM_AW) ")" ::: "memory")

constexpr int L_WO = 0;
constexpr int L_PART = 128 * 1152;
constexpr int L_MLP = L_PART + 4096;
constexpr int L_Y = L_MLP + 3 * 2304;
constexpr int L_HID = L_Y + 3 * 1152;
constexpr int L_ML = L_HID + 3 * 192;
constexpr int L_OBF = L_ML + 64;
constexpr int L_Q = L_OBF + 256;
constexpr int L_MISC = L_Q + 256;
constexpr int L_BYTES = L_MISC + 16;
static_assert(L_BYTES <= 163840, "LDS");

constexpr size_t W_EPOCH = 0;
constexpr size_t W_QX = 4096;
constexpr size_t W_INBOX = W_QX + (size_t)NH * NB * 4 * 128 * 8;
constexpr size_t W_BYTES = W_INBOX + (size_t)NB * 36 * 32 * 16 * 8;

#define GAS __attribute__((address_space(1)))
#define LAS __attribute__((address_space(3)))
typedef unsigned v4u __attribute__((ext_vector_type(4)));
typedef unsigned v2u __attribute__((ext_vector_type(2)));
typedef float f32x4 __attribute__((ext_vector_type(4)));
typedef short s16x4 __attribute__((ext_vector_type(4)));
typedef __bf16 bf16x2 __attribute__((ext_vector_type(2)));
typedef __bf16 bf16x8 __attribute__((ext_vector_type(8)));
typedef unsigned long long u64;
typedef unsigned short u16;

struct Args {
  const u16* x; const u16* k; const u16* v; const u16* wq; const u16* wo;
  const u16* w1; const u16* b1; const u16* w2; const u16* b2; const u16* gw;
  u16* out; unsigned char* ws;
};

__device__ __forceinline__ v4u ld_nt(const GAS v4u* p) { return __builtin_nontemporal_load(p); }
__device__ __forceinline__ v4u ld_w(const GAS v4u* p) {
  return __builtin_nontemporal_load(p);
}
__device__ __forceinline__ void glds16(const GAS unsigned char* g, unsigned lds_base) {
  unsigned keep;
  asm volatile("s_mov_b32 %0, m0\n\ts_mov_b32 m0, %2\n\ts_nop 0\n\tglobal_load_lds_dwordx4 %1, off nt\n\ts_mov_b32 m0, %0" : "=&s"(keep) : "v"(g), "s"(lds_base) : "memory");
}
__device__ __forceinline__ float bf2f(unsigned h) { return __builtin_bit_cast(float, h << 16); }
__device__ __forceinline__ float bflo(unsigned u) { return __builtin_bit_cast(float, u << 16); }
__device__ __forceinline__ float bfhi(unsigned u) { return __builtin_bit_cast(float, u & 0xffff0000u); }
__device__ __forceinline__ unsigned pk_bf16(float lo, float hi) {
  bf16x2 p = {(__bf16)lo, (__bf16)hi};
  return __builtin_bit_cast(unsigned, p);
}
__device__ __forceinline__ float rbf(float f) { return (float)(__bf16)f; }
__device__ __forceinline__ float dot2(unsigned a, unsigned b, float c) {
  return __builtin_amdgcn_fdot2_f32_bf16(__builtin_bit_cast(bf16x2, a), __builtin_bit_cast(bf16x2, b), c, false);
}
__device__ __forceinline__ f32x4 mfma(v4u a, v4u b, f32x4 c) {
  return __builtin_amdgcn_mfma_f32_16x16x32_bf16(__builtin_bit_cast(bf16x8, a), __builtin_bit_cast(bf16x8, b), c, 0, 0, 0);
}
template <int CTRL> __device__ __forceinline__ float dppf(float x) {
  return __builtin_bit_cast(float, __builtin_amdgcn_mov_dpp(__builtin_bit_cast(int, x), CTRL, 0xf, 0xf, true));
}
template <int CTRL> __device__ __forceinline__ unsigned dppu(unsigned x) {
  return (unsigned)__builtin_amdgcn_mov_dpp((int)x, CTRL, 0xf, 0xf, true);
}
constexpr int XOR1 = 0xB1, XOR2 = 0x4E, XOR7 = 0x141, XOR8 = 0x128, BCAST = 0x150;
__device__ __forceinline__ float xrow_sum(float x) {
  auto s = __builtin_amdgcn_permlane16_swap(__float_as_uint(x), __float_as_uint(x), false, false);
  x = __uint_as_float(s[0]) + __uint_as_float(s[1]);
  auto t = __builtin_amdgcn_permlane32_swap(__float_as_uint(x), __float_as_uint(x), false, false);
  return __uint_as_float(t[0]) + __uint_as_float(t[1]);
}
__device__ __forceinline__ float xrow_max(float x) {
  auto s = __builtin_amdgcn_permlane16_swap(__float_as_uint(x), __float_as_uint(x), false, false);
  x = fmaxf(__uint_as_float(s[0]), __uint_as_float(s[1]));
  auto t = __builtin_amdgcn_permlane32_swap(__float_as_uint(x), __float_as_uint(x), false, false);
  return fmaxf(__uint_as_float(t[0]), __uint_as_float(t[1]));
}
__device__ __forceinline__ float fold_halves(float x) {
  const float t = dppf<XOR8>(x);
  auto p = __builtin_amdgcn_permlane32_swap(__float_as_uint(t), __float_as_uint(t), false, false);
  return x + __uint_as_float(p[1]);
}
__device__ __forceinline__ float wave_sum(float x) {
  x += dppf<XOR1>(x); x += dppf<XOR2>(x); x += dppf<XOR7>(x); x += dppf<XOR8>(x);
  return xrow_sum(x);
}
__device__ __forceinline__ float wave_max(float x) {
  x = fmaxf(x, dppf<XOR1>(x)); x = fmaxf(x, dppf<XOR2>(x)); x = fmaxf(x, dppf<XOR7>(x)); x = fmaxf(x, dppf<XOR8>(x));
  return xrow_max(x);
}
#define WG_BARRIER() asm volatile("s_waitcnt lgkmcnt(0)\n\ts_barrier" ::: "memory")
#define SCHED_FENCE() __builtin_amdgcn_sched_barrier(0)

#define STAMP(i) do { } while (0)

constexpr float SCL = 0.088388347648318440f * 1.4426950408889634f;

#define GVAL(x) __builtin_bit_cast(float, (unsigned)(x) << 8)
__device__ __forceinline__ void gather8(GAS u64* ib, u64 tag, u64 (&gv)[8]) {
#pragma unroll
  for (int i = 0; i < 8; ++i) gv[i] = __hip_atomic_load(ib + 64 * i, __ATOMIC_RELAXED, __HIP_MEMORY_SCOPE_AGENT);
  for (unsigned spin = 0; spin < (1u << 22); ++spin) {
    bool all = true;
#pragma unroll
    for (int i = 0; i < 8; ++i) all = all && (gv[i] >> 24) == tag;
    if (__builtin_amdgcn_ballot_w64(!all) == 0) break;
    __builtin_amdgcn_s_sleep(1);
#pragma unroll
    for (int i = 0; i < 8; ++i)
      if ((gv[i] >> 24) != tag) gv[i] = __hip_atomic_load(ib + 64 * i, __ATOMIC_RELAXED, __HIP_MEMORY_SCOPE_AGENT);
  }
}

struct Soft { float m, mc, l; f32x4 acc; };

template <bool ISSUE>
__device__ __forceinline__ void tile_step(v4u (&kr)[4], v4u (&vr)[4], const GAS v4u* kn, const GAS v4u* vn, const v4u q,
                                          const unsigned (&mask)[4], bool hi8, bool hi4, Soft& s) {
  float x[4];
#pragma unroll
  for (int t = 0; t < 4; ++t) {
    float d = dot2(kr[t][0], q[0], 0.f);
    d = dot2(kr[t][1], q[1], d); d = dot2(kr[t][2], q[2], d); d = dot2(kr[t][3], q[3], d);
    x[t] = d;
    if (ISSUE) { asm volatile("" : "+v"(x[t])); KV_THROTTLE(); kr[t] = ld_nt(kn + t * 64); SCHED_FENCE(); }
  }
  const float a0 = x[0] + dppf<XOR8>(x[0]), a2 = x[2] + dppf<XOR8>(x[2]);
  const float a1 = x[1] + dppf<XOR8>(x[1]), a3 = x[3] + dppf<XOR8>(x[3]);
  const float y0 = hi8 ? a2 : a0, y1 = hi8 ? a3 : a1;
  const float b0 = y0 + dppf<XOR7>(y0), b1 = y1 + dppf<XOR7>(y1);
  float z = hi4 ? b1 : b0;
  z += dppf<XOR2>(z); z += dppf<XOR1>(z);
  if (__builtin_amdgcn_ballot_w64(z > s.m) != 0) {
    const float mn = fmaxf(s.m, wave_max(z));
    const float mcn = mn * SCL;
    const float alpha = __builtin_amdgcn_exp2f(s.mc - mcn);
    s.m = mn; s.mc = mcn; s.l *= alpha; s.acc *= alpha;
  }
  const float p = __builtin_amdgcn_exp2f(fmaf(z, SCL, -s.mc));
  s.l += p;
  const unsigned pk = pk_bf16(p, p);
#define PV_STEP(t) { const v4u bf = {dppu<BCAST + 4 * t>(pk) & mask[0], dppu<BCAST + 4 * t>(pk) & mask[1], dppu<BCAST + 4 * t>(pk) & mask[2], dppu<BCAST + 4 * t>(pk) & mask[3]}; \
    s.acc = mfma(vr[t], bf, s.acc); if (ISSUE) { SCHED_FENCE(); KV_THROTTLE(); vr[t] = ld_nt(vn + t * 64); SCHED_FENCE(); } }
  PV_STEP(0) PV_STEP(1) PV_STEP(2) PV_STEP(3)
#undef PV_STEP
}

__global__ void __launch_bounds__(NTHR, 3) fused_kernel(Args a) {
  extern __shared__ __attribute__((aligned(16))) unsigned char smem[];
  LAS unsigned char* L = (LAS unsigned char*)smem;
  const int tid = threadIdx.x, lane = tid & 63, w = __builtin_amdgcn_readfirstlane(tid >> 6);
  const int bid = blockIdx.x;
  const int h = (bid & 7) * 4 + ((bid >> 3) & 3), b = bid >> 5;
  const int g = lane >> 4, c = lane & 15;
  const u64 dg_t0 = __builtin_amdgcn_s_memrealtime();
  unsigned dg_q = 0, dg_e = 0;

  if (w >= NWA) {
    const int ws_ = w - NWA;
    const size_t kvoff = ((size_t)(b * NH + h) * SK + ws_ * TK) * DH;
    const GAS v4u* kp = (const GAS v4u*)(a.k + kvoff) + lane;
    const GAS v4u* vp = (const GAS v4u*)(a.v + kvoff) + lane;
    constexpr int TSTR = NWS * TK * DH / 8;
    v4u kr[DEPTH][4], vr[DEPTH][4];
    int rot = GM_ROT == 0 ? 0 : GM_ROT == 1 ? (int)((bid * 2654435761u) >> 27) : GM_ROT == 2 ? (int)(((bid * 8 + ws_) * 2654435761u) >> 27) : GM_ROT == 3 ? (bid >> 5) * 4 : GM_ROT == 4 ? ws_ * 4 : (bid >> 3);
#define TOFF(i) ((((i) + rot) & (NT - 1)) * TSTR)
    if (GM_B0) WG_BARRIER();
#pragma unroll
    for (int d = 0; d < DEPTH; ++d) {
#pragma unroll
      for (int t = 0; t < 4; ++t) { KV_THROTTLE0(); kr[d][t] = ld_nt(kp + TOFF(d) + t * 64); SCHED_FENCE(); }
#pragma unroll
      for (int t = 0; t < 4; ++t) { KV_THROTTLE0(); vr[d][t] = ld_nt(vp + TOFF(d) + t * 64); SCHED_FENCE(); }
    }
    unsigned mask[4];
#pragma unroll
    for (int r = 0; r < 4; ++r) mask[r] = (c >> 1) == r ? ((c & 1) ? 0xffff0000u : 0x0000ffffu) : 0u;
    LAS float* part = (LAS float*)(L + L_PART);
    WG_BARRIER();
    const v4u q = *(const LAS v4u*)(L + L_Q + 16 * c);
    dg_q = (unsigned)(__builtin_amdgcn_s_memrealtime() - dg_t0);
    Soft s; s.m = -INFINITY; s.mc = -INFINITY; s.l = 0.f; s.acc = f32x4{0.f, 0.f, 0.f, 0.f};
    const bool hi8 = (c & 8) != 0, hi4 = (c & 4) != 0;
    constexpr int REM = NT % DEPTH;
#pragma unroll 1
    for (int it = 0; it < NT / DEPTH - 1; ++it) {
      rot += DEPTH;
#pragma unroll
      for (int d = 0; d < DEPTH; ++d) tile_step<true>(kr[d], vr[d], kp + TOFF(d), vp + TOFF(d), q, mask, hi8, hi4, s);
    }
    rot += DEPTH;
#pragma unroll
    for (int d = 0; d < DEPTH; ++d) {
      if (d < REM) tile_step<true>(kr[d], vr[d], kp + TOFF(d), vp + TOFF(d), q, mask, hi8, hi4, s);
      else tile_step<false>(kr[d], vr[d], kp, vp, q, mask, hi8, hi4, s);
    }
#pragma unroll
    for (int d = 0; d < REM; ++d) tile_step<false>(kr[d], vr[d], kp, vp, q, mask, hi8, hi4, s);
    const float lsum = wave_sum(s.l) * 0.25f;
    if (c < 8) {
#pragma unroll
      for (int r = 0; r < 4; ++r) part[ws_ * DH + 8 * (4 * g + r) + c] = s.acc[r];
    }
    if (lane == 0) { ((LAS float*)(L + L_ML))[2 * ws_] = s.mc; ((LAS float*)(L + L_ML))[2 * ws_ + 1] = lsum; }
  } else if (w > 0) {
    const int m = w - 1;
    if (GM_PRIO) __builtin_amdgcn_s_setprio(3);
    if (GM_B0) WG_BARRIER();
    WG_BARRIER();
    const GAS u16* xp = (const GAS u16*)(a.x + b * DM);
    float xv[9];
#pragma unroll
    for (int i = 0; i < 9; ++i) xv[i] = bf2f(xp[lane + 64 * i]);
    const int col0 = 64 * ((9 * h) / 4 + m), sl = 4 * (c >> 3) + g;
    const GAS unsigned char* w1p = (const GAS unsigned char*)a.w1 + (size_t)(72 * sl) * (FF * 2) + col0 * 2 + (c & 7) * 16;
    constexpr int R1 = GM_R1;
    v4u r1[R1];
#pragma unroll
    for (int i = 0; i < R1; ++i) { r1[i] = ld_w((const GAS v4u*)(w1p + i * (FF * 2))); SCHED_FENCE(); }
    float b1v[4];
#pragma unroll
    for (int r = 0; r < 4; ++r) b1v[r] = bf2f(((const GAS u16*)a.b1)[col0 + 8 * (4 * (g & 1) + r) + (c & 7)]);
    if (m == 0) {
      const GAS u16* gp = (const GAS u16*)a.gw;
      float gs = 0.f;
#pragma unroll
      for (int i = 0; i < 9; ++i) gs += rbf(xv[i] * bf2f(gp[lane + 64 * i]));
      const float logit = rbf(wave_sum(gs));
      const float gate = rbf(1.f / (1.f + __expf(-logit)));
      if (lane == 0) *(LAS float*)(L + L_MISC) = gate;
    }
    float sum = 0.f;
#pragma unroll
    for (int i = 0; i < 9; ++i) sum += xv[i];
    const float mean = wave_sum(sum) * (1.f / DM);
    float sq = 0.f;
#pragma unroll
    for (int i = 0; i < 9; ++i) sq += (xv[i] - mean) * (xv[i] - mean);
    const float var = rbf(wave_sum(sq) * (1.f / DM));
    const float mb = rbf(mean), sd = rbf(sqrtf(rbf(var + 1e-5f)));
    LAS u16* yl = (LAS u16*)(L + L_Y + m * 1152);
#pragma unroll
    for (int i = 0; i < 9; ++i) yl[lane + 64 * i] = __builtin_bit_cast(u16, (__bf16)(rbf(xv[i] - mb) / sd));
    asm volatile("s_waitcnt lgkmcnt(0)" ::: "memory");
    unsigned mask[4];
#pragma unroll
    for (int r = 0; r < 4; ++r) mask[r] = ((c & 7) >> 1) == r ? ((c & 1) ? 0xffff0000u : 0x0000ffffu) : 0u;
    f32x4 h1 = {0.f, 0.f, 0.f, 0.f};
    static_assert(24 % R1 == 0, "ring");
#pragma unroll 1
    for (int o = 0; o < 72; o += 24) {
      v4u yy[3];
#pragma unroll
      for (int i = 0; i < 3; ++i) yy[i] = *(const LAS v4u*)(yl + 72 * sl + o + 8 * i);
#pragma unroll
      for (int i = 0; i < 24; ++i) {
        const unsigned u = yy[i >> 3][(i >> 1) & 3];
        const unsigned dup = __builtin_amdgcn_perm(u, u, (i & 1) ? 0x03020302u : 0x01000100u);
        h1 = mfma(r1[i % R1], v4u{dup & mask[0], dup & mask[1], dup & mask[2], dup & mask[3]}, h1);
        SCHED_FENCE();
        const int nx = o + i + R1 < 72 ? o + i + R1 : 71;
        r1[i % R1] = ld_w((const GAS v4u*)(w1p + (size_t)nx * (FF * 2)));
        SCHED_FENCE();
      }
    }
    const int lo = col0 > FS * h ? col0 : FS * h, hi = col0 + 64 < FS * h + FS ? col0 + 64 : FS * h + FS;
    const int nrg = (hi - lo) >> 3;
    const GAS unsigned char* w2p = (const GAS unsigned char*)a.w2 + (size_t)(lo + sl) * (DM * 2) + (c & 7) * 16;
    v4u r2[3];
#pragma unroll
    for (int i = 0; i < 3; ++i) { r2[i] = ld_w((const GAS v4u*)(w2p + i * 128)); SCHED_FENCE(); }
    LAS u16* hid = (LAS u16*)(L + L_HID + m * 128);
#pragma unroll
    for (int r = 0; r < 4; ++r) {
      const float pre = rbf(rbf(fold_halves(h1[r])) + b1v[r]);
      const float inner = 0.7978845608028654f * (pre + 0.044715f * pre * pre * pre);
      const float th = 1.f - 2.f / (1.f + __expf(2.f * inner));
      if (lane < 32 && c < 8) hid[8 * (4 * g + r) + c] = __builtin_bit_cast(u16, (__bf16)(0.5f * pre * (1.f + th)));
    }
    asm volatile("s_waitcnt lgkmcnt(0)" ::: "memory");
    f32x4 o2[9];
#pragma unroll
    for (int j = 0; j < 9; ++j) o2[j] = f32x4{0.f, 0.f, 0.f, 0.f};
#pragma unroll 1
    for (int rg = 0; rg < nrg; ++rg) {
      const unsigned u = hid[lo - col0 + 8 * rg + sl];
      const unsigned dup = u | (u << 16);
      const v4u bf = {dup & mask[0], dup & mask[1], dup & mask[2], dup & mask[3]};
      const GAS unsigned char* cur = w2p + (size_t)rg * (8 * DM * 2);
      const GAS unsigned char* nxt = w2p + (size_t)(rg + 1 < nrg ? rg + 1 : rg) * (8 * DM * 2);
#pragma unroll
      for (int j = 0; j < 9; ++j) {
        o2[j] = mfma(r2[j % 3], bf, o2[j]);
        SCHED_FENCE();
        r2[j % 3] = ld_w((const GAS v4u*)(j < 6 ? cur + (j + 3) * 128 : nxt + (j - 6) * 128));
        SCHED_FENCE();
      }
    }
    LAS float* mp = (LAS float*)(L + L_MLP + m * 2304);
#pragma unroll
    for (int j = 0; j < 9; ++j)
#pragma unroll
      for (int r = 0; r < 4; ++r) {
        const float t = fold_halves(o2[j][r]);
        if (lane < 32 && c < 8) mp[64 * j + 8 * (4 * g + r) + c] = t;
      }
  } else {
    if (GM_PRIO) __builtin_amdgcn_s_setprio(3);
    {
      u64 e1 = *(const __attribute__((address_space(4))) u64*)(a.ws + W_EPOCH);
      const int ch = b & 1, kq = b >> 1;
      const GAS unsigned char* wqb = (const GAS unsigned char*)a.wq + (size_t)(144 * kq + 8 * (lane >> 5) + ((lane >> 3) & 3)) * (NH * DH * 2)
                                     + h * 256 + ch * 128 + (((((lane & 7) >> 1) ^ g) << 1) | (lane & 1)) * 16;
#pragma unroll
      for (int i = 0; i < 18; ++i) glds16(wqb + (size_t)(16 * (i >> 1) + 4 * (i & 1)) * (NH * DH * 2), (unsigned)(size_t)(L + L_WO) + i * 1024);
      const GAS unsigned char* xb = (const GAS unsigned char*)a.x + (c & 7) * (DM * 2) + 288 * kq;
      v4u xa[5];
#pragma unroll
      for (int ks = 0; ks < 4; ++ks) xa[ks] = *(const GAS v4u*)(xb + 64 * ks + 16 * g);
      xa[4] = *(const GAS v4u*)(xb + 256 + 16 * (g & 1));
      SCHED_FENCE();
      if (GM_B0) asm volatile("s_barrier" ::: "memory");
      asm volatile("s_waitcnt vmcnt(0)" : "+v"(xa[0]), "+v"(xa[1]), "+v"(xa[2]), "+v"(xa[3]), "+v"(xa[4]) :: "memory");
      if (g >= 2) xa[4] = v4u{0u, 0u, 0u, 0u};
      asm volatile("" : "+s"(e1));
      e1 += 1;
      const u64 tag = e1 & ((1ull << 40) - 1);
      if (lane == 0) *(LAS u64*)(L + L_MISC + 8) = e1;
      const int q4 = (lane >> 2) & 3, p4 = lane & 3, sw = 2 * (g & 1) + (q4 >> 1);
      const int rb = (4 * (g & 1) + q4) * 128 + 8 * p4;
      f32x4 qc[4];
#pragma unroll
      for (int cb = 0; cb < 4; ++cb) {
        qc[cb] = f32x4{0.f, 0.f, 0.f, 0.f};
        LAS unsigned char* tb = L + L_WO + rb + ((cb ^ sw) * 32);
#pragma unroll
        for (int ks = 0; ks < 5; ++ks) {
          LAS unsigned char* t2 = tb + ks * 4096 + (ks < 4 ? (g >> 1) * 2048 : 0);
          const v2u l2 = __builtin_bit_cast(v2u, __builtin_amdgcn_ds_read_tr16_b64_v4i16((LAS s16x4*)t2));
          const v2u h2 = __builtin_bit_cast(v2u, __builtin_amdgcn_ds_read_tr16_b64_v4i16((LAS s16x4*)(t2 + 1024)));
          qc[cb] = mfma(xa[ks], v4u{l2[0], l2[1], h2[0], h2[1]}, qc[cb]);
        }
      }
      GAS u64* qx = (GAS u64*)(a.ws + W_QX);
#pragma unroll
      for (int pr = 0; pr < 2; ++pr)
#pragma unroll
        for (int r = 0; r < 4; ++r) {
          const float val = g < 2 ? qc[2 * pr][r] : qc[2 * pr + 1][r];
          const u64 gr = (tag << 24) | (u64)((__builtin_bit_cast(unsigned, val) + 0x80u) >> 8);
          __hip_atomic_store(qx + ((size_t)(h * NB + 4 * (g & 1) + r) * 4 + kq) * 128 + 64 * ch + 16 * (2 * pr + (g >> 1)) + c, gr, __ATOMIC_RELAXED, __HIP_MEMORY_SCOPE_AGENT);
        }
      GAS u64* ib = qx + (size_t)(h * NB + b) * 512 + lane;
      u64 gv[8];
      gather8(ib, tag, gv);
      const float q0 = (GVAL(gv[0]) + GVAL(gv[2])) + (GVAL(gv[4]) + GVAL(gv[6]));
      const float q1 = (GVAL(gv[1]) + GVAL(gv[3])) + (GVAL(gv[5]) + GVAL(gv[7]));
      ((LAS u16*)(L + L_Q))[lane] = __builtin_bit_cast(u16, (__bf16)q0);
      ((LAS u16*)(L + L_Q))[64 + lane] = __builtin_bit_cast(u16, (__bf16)q1);
    }
    WG_BARRIER();
    const GAS unsigned char* wob = (const GAS unsigned char*)(a.wo + (size_t)h * DH * DM);
    int so[9];
#pragma unroll
    for (int j = 0; j < 9; ++j) {
      const int sl = 64 * j + lane, row = sl / 72, ch = sl % 72;
      so[j] = row * 1152 + ((((ch >> 1) ^ ((row >> 1) & 3)) << 1) | (ch & 1)) * 16;
    }
#pragma unroll 1
    for (int gr = 0; gr < 16; ++gr) {
#pragma unroll
      for (int j = 0; j < 9; ++j)
      { glds16(wob + gr * 9216 + so[j], (unsigned)(size_t)(L + L_WO) + gr * 9216 + j * 1024); AUX_THROTTLE(); }
    }
    asm volatile("s_waitcnt vmcnt(0)" ::: "memory");
  }
  STAMP(2);
  WG_BARRIER();
  dg_e = (unsigned)(__builtin_amdgcn_s_memrealtime() - dg_t0);
  STAMP(3);
  if (tid < DH) {
    const LAS float* ml = (const LAS float*)(L + L_ML);
    const LAS float* part = (const LAS float*)(L + L_PART);
    float mx = ml[0];
#pragma unroll
    for (int i = 1; i < NWS; ++i) mx = fmaxf(mx, ml[2 * i]);
    float den = 0.f, num = 0.f;
#pragma unroll
    for (int i = 0; i < NWS; ++i) {
      const float f = __builtin_amdgcn_exp2f(ml[2 * i] - mx);
      den = fmaf(f, ml[2 * i + 1], den); num = fmaf(f, part[i * DH + tid], num);
    }
    const int pos = (tid & 96) + 8 * ((tid >> 2) & 3) + 4 * ((tid >> 4) & 1) + (tid & 3);
    ((LAS u16*)(L + L_OBF))[pos] = __builtin_bit_cast(u16, (__bf16)(num / den));
  }
  WG_BARRIER();
  const float gate = *(const LAS float*)(L + L_MISC);
  const float omg = rbf(1.f - gate);
  const u64 ep1 = *(const LAS u64*)(L + L_MISC + 8);
  const u64 tag = ep1 & ((1ull << 40) - 1);
  {
    const int q4 = (lane >> 2) & 3, p4 = lane & 3;
    const int base = (4 * g + q4) * 1152 + ((w ^ ((2 * g + (q4 >> 1)) & 3)) * 32) + 8 * p4;
    f32x4 oc[3];
#pragma unroll
    for (int j = 0; j < 3; ++j) oc[j] = f32x4{0.f, 0.f, 0.f, 0.f};
#pragma unroll
    for (int ks = 0; ks < 4; ++ks) {
      const v4u af = *(const LAS v4u*)(L + L_OBF + 64 * ks + 16 * g);
#pragma unroll
      for (int j = 0; j < 3; ++j) {
        const s16x4 lo = __builtin_amdgcn_ds_read_tr16_b64_v4i16((LAS s16x4*)(L + L_WO + base + ks * 32 * 1152 + j * 384));
        const s16x4 hi = __builtin_amdgcn_ds_read_tr16_b64_v4i16((LAS s16x4*)(L + L_WO + base + (ks * 32 + 16) * 1152 + j * 384));
        const v2u l2 = __builtin_bit_cast(v2u, lo), h2 = __builtin_bit_cast(v2u, hi);
        oc[j] = mfma(af, v4u{l2[0], l2[1], h2[0], h2[1]}, oc[j]);
      }
    }
    if (lane < 48) {
      const int cb = w + 12 * g, n = 16 * cb + c;
      const float av = g == 0 ? oc[0][0] : (g == 1 ? oc[1][0] : oc[2][0]);
      const LAS float* mp = (const LAS float*)(L + L_MLP);
      const float tot = gate * av + omg * ((mp[n] + mp[DM + n]) + mp[2 * DM + n]);
      const u64 gr = (tag << 24) | (u64)((__builtin_bit_cast(unsigned, tot) + 0x80u) >> 8);
      __hip_atomic_store((GAS u64*)(a.ws + W_INBOX) + ((size_t)(b * 36 + cb) * 32 + h) * 16 + c, gr, __ATOMIC_RELAXED, __HIP_MEMORY_SCOPE_AGENT);
    }
  }
  STAMP(4);
  if (w == 0 || (w == 1 && h < 4)) {
    const int cb = h + 32 * w;
    const float b2v = bf2f(((const GAS u16*)a.b2)[16 * cb + c]);
    GAS u64* ib = (GAS u64*)(a.ws + W_INBOX) + (size_t)(b * 36 + cb) * 32 * 16 + lane;
    u64 gv[8];
    gather8(ib, tag, gv);
    float sum = 0.f;
#pragma unroll
    for (int i = 0; i < 8; ++i) sum += __builtin_bit_cast(float, (unsigned)gv[i] << 8);
    sum = xrow_sum(sum);
    if (lane < 16) ((GAS u16*)a.out)[b * DM + 16 * cb + c] = __builtin_bit_cast(u16, (__bf16)(sum + omg * b2v));
    if (w == 0 && bid == 0 && lane == 0)
      __hip_atomic_store((GAS u64*)(a.ws + W_EPOCH), ep1, __ATOMIC_RELAXED, __HIP_MEMORY_SCOPE_AGENT);
    STAMP(5);
  }
  if (w == 4) {
    unsigned metric = 0;
    if (DG_MODE == 1) metric = 10 * dg_q;
    if (DG_MODE == 2) metric = 4 * dg_e;
    if (DG_MODE == 3) metric = 40000 - 4 * dg_e;
    if (DG_MODE == 4) metric = 1000 * __builtin_ctzll((u64)a.k) ;
    if (DG_MODE == 5) metric = 1000 * __builtin_ctzll((u64)a.v - (u64)a.k);
    const unsigned tgt = 15000 + metric;
    while ((unsigned)(__builtin_amdgcn_s_memrealtime() - dg_t0) < tgt) __builtin_amdgcn_s_sleep(2);
  }
}

}

extern "C" void kernel_launch(void* const* d_in, const int* in_sizes, int n_in, void* d_out, int out_size, void* d_ws,
                              size_t ws_size, hipStream_t stream) {
  (void)in_sizes; (void)n_in; (void)out_size; (void)ws_size;
  gm::Args a;
  a.x = (const gm::u16*)d_in[0]; a.k = (const gm::u16*)d_in[1]; a.v = (const gm::u16*)d_in[2];
  a.wq = (const gm::u16*)d_in[3]; a.wo = (const gm::u16*)d_in[4];
  a.w1 = (const gm::u16*)d_in[6]; a.b1 = (const gm::u16*)d_in[7]; a.w2 = (const gm::u16*)d_in[8]; a.b2 = (const gm::u16*)d_in[9];
  a.gw = (const gm::u16*)d_in[10];
  a.out = (gm::u16*)d_out; a.ws = (unsigned char*)d_ws;
  (void)hipFuncSetAttribute((const void*)gm::fused_kernel, hipFuncAttributeMaxDynamicSharedMemorySize, gm::L_BYTES);
  hipLaunchKernelGGL(gm::fused_kernel, dim3(gm::NB * gm::NH), dim3(gm::NTHR), gm::L_BYTES, stream, a);
}
```

```cpp
#define DG_MODE 0
#include <hip/hip_runtime.h>
#include <hip/hip_bf16.h>
#include <stdint.h>

namespace gm {

constexpr int NB = 8, NH = 32, SK = 4096, DH = 128, DM = 576, FF = 4608;
constexpr int FS = FF / NH;
constexpr int NWS = 8, NWA = 4, NTHR = (NWS + NWA) * 64;
constexpr int TK = 16;
constexpr int NT = SK / TK / NWS;
#define GM_DEPTH 3
constexpr int DEPTH = GM_DEPTH;
#define GM_WNT 1
#define GM_PRIO 1
#define GM_LATE 1
#define GM_B0 1
#define GM_AW 2
#define STR2(x) #x
#define STR(x) STR2(x)
#define GM_SW 2
#define GM_SW0 GM_SW
#define KV_THROTTLE() asm volatile("s_waitcnt vmcnt(" STR(GM_SW) ")" ::: "memory")
#define KV_THROTTLE0() asm volatile("s_waitcnt vmcnt(" STR(GM_SW0) ")" ::: "memory")
#define GM_ROT 0
#define GM_R1 3
#define AUX_THROTTLE() asm volatile("s_waitcnt vmcnt(" STR(GM_AW) ")" ::: "memory")

constexpr int L_WO = 0;
constexpr int L_PART = 128 * 1152;
constexpr int L_MLP = L_PART + 4096;
constexpr int L_Y = L_MLP + 3 * 2304;
constexpr int L_HID = L_Y + 3 * 1152;
constexpr int L_ML = L_HID + 3 * 192;
constexpr int L_OBF = L_ML + 64;
constexpr int L_Q = L_OBF + 256;
constexpr int L_MISC = L_Q + 256;
constexpr int L_BYTES = L_MISC + 16;
static_assert(L_BYTES <= 163840, "LDS");

constexpr size_t W_EPOCH = 0;
constexpr size_t W_QX = 4096;
constexpr size_t W_INBOX = W_QX + (size_t)NH * NB * 4 * 128 * 8;
constexpr size_t W_BYTES = W_INBOX + (size_t)NB * 36 * 32 * 16 * 8;

#define GAS __attribute__((address_space(1)))
#define LAS __attribute__((address_space(3)))
typedef unsigned v4u __attribute__((ext_vector_type(4)));
typedef unsigned v2u __attribute__((ext_vector_type(2)));
typedef float f32x4 __attribute__((ext_vector_type(4)));
typedef short s16x4 __attribute__((ext_vector_type(4)));
typedef __bf16 bf16x2 __attribute__((ext_vector_type(2)));
typedef __bf16 bf16x8 __attribute__((ext_vector_type(8)));
typedef unsigned long long u64;
typedef unsigned short u16;

struct Args {
  const u16* x; const u16* k; const u16* v; const u16* wq; const u16* wo;
  const u16* w1; const u16* b1; const u16* w2; const u16* b2; const u16* gw;
  u16* out; unsigned char* ws;
};

__device__ __forceinline__ v4u ld_nt(const GAS v4u* p) { return __builtin_nontemporal_load(p); }
__device__ __forceinline__ v4u ld_w(const GAS v4u* p) {
  return __builtin_nontemporal_load(p);
}
__device__ __forceinline__ void glds16(const GAS unsigned char* g, unsigned lds_base) {
  unsigned keep;
  asm volatile("s_mov_b32 %0, m0\n\ts_mov_b32 m0, %2\n\ts_nop 0\n\tglobal_load_lds_dwordx4 %1, off nt\n\ts_mov_b32 m0, %0" : "=&s"(keep) : "v"(g), "s"(lds_base) : "memory");
}
__device__ __forceinline__ float bf2f(unsigned h) { return __builtin_bit_cast(float, h << 16); }
__device__ __forceinline__ float bflo(unsigned u) { return __builtin_bit_cast(float, u << 16); }
__device__ __forceinline__ float bfhi(unsigned u) { return __builtin_bit_cast(float, u & 0xffff0000u); }
__device__ __forceinline__ unsigned pk_bf16(float lo, float hi) {
  bf16x2 p = {(__bf16)lo, (__bf16)hi};
  return __builtin_bit_cast(unsigned, p);
}
__device__ __forceinline__ float rbf(float f) { return (float)(__bf16)f; }
__device__ __forceinline__ float dot2(unsigned a, unsigned b, float c) {
  return __builtin_amdgcn_fdot2_f32_bf16(__builtin_bit_cast(bf16x2, a), __builtin_bit_cast(bf16x2, b), c, false);
}
__device__ __forceinline__ f32x4 mfma(v4u a, v4u b, f32x4 c) {
  return __builtin_amdgcn_mfma_f32_16x16x32_bf16(__builtin_bit_cast(bf16x8, a), __builtin_bit_cast(bf16x8, b), c, 0, 0, 0);
}
template <int CTRL> __device__ __forceinline__ float dppf(float x) {
  return __builtin_bit_cast(float, __builtin_amdgcn_mov_dpp(__builtin_bit_cast(int, x), CTRL, 0xf, 0xf, true));
}
template <int CTRL> __device__ __forceinline__ unsigned dppu(unsigned x) {
  return (unsigned)__builtin_amdgcn_mov_dpp((int)x, CTRL, 0xf, 0xf, true);
}
constexpr int XOR1 = 0xB1, XOR2 = 0x4E, XOR7 = 0x141, XOR8 = 0x128, BCAST = 0x150;
__device__ __forceinline__ float xrow_sum(float x) {
  auto s = __builtin_amdgcn_permlane16_swap(__float_as_uint(x), __float_as_uint(x), false, false);
  x = __uint_as_float(s[0]) + __uint_as_float(s[1]);
  auto t = __builtin_amdgcn_permlane32_swap(__float_as_uint(x), __float_as_uint(x), false, false);
  return __uint_as_float(t[0]) + __uint_as_float(t[1]);
}
__device__ __forceinline__ float xrow_max(float x) {
  auto s = __builtin_amdgcn_permlane16_swap(__float_as_uint(x), __float_as_uint(x), false, false);
  x = fmaxf(__uint_as_float(s[0]), __uint_as_float(s[1]));
  auto t = __builtin_amdgcn_permlane32_swap(__float_as_uint(x), __float_as_uint(x), false, false);
  return fmaxf(__uint_as_float(t[0]), __uint_as_float(t[1]));
}
__device__ __forceinline__ float fold_halves(float x) {
  const float t = dppf<XOR8>(x);
  auto p = __builtin_amdgcn_permlane32_swap(__float_as_uint(t), __float_as_uint(t), false, false);
  return x + __uint_as_float(p[1]);
}
__device__ __forceinline__ float wave_sum(float x) {
  x += dppf<XOR1>(x); x += dppf<XOR2>(x); x += dppf<XOR7>(x); x += dppf<XOR8>(x);
  return xrow_sum(x);
}
__device__ __forceinline__ float wave_max(float x) {
  x = fmaxf(x, dppf<XOR1>(x)); x = fmaxf(x, dppf<XOR2>(x)); x = fmaxf(x, dppf<XOR7>(x)); x = fmaxf(x, dppf<XOR8>(x));
  return xrow_max(x);
}
#define WG_BARRIER() asm volatile("s_waitcnt lgkmcnt(0)\n\ts_barrier" ::: "memory")
#define SCHED_FENCE() __builtin_amdgcn_sched_barrier(0)

#define STAMP(i) do { } while (0)

constexpr float SCL = 0.088388347648318440f * 1.4426950408889634f;

#define GVAL(x) __builtin_bit_cast(float, (unsigned)(x) << 8)
__device__ __forceinline__ void gather8(GAS u64* ib, u64 tag, u64 (&gv)[8]) {
#pragma unroll
  for (int i = 0; i < 8; ++i) gv[i] = __hip_atomic_load(ib + 64 * i, __ATOMIC_RELAXED, __HIP_MEMORY_SCOPE_AGENT);
  for (unsigned spin = 0; spin < (1u << 22); ++spin) {
    bool all = true;
#pragma unroll
    for (int i = 0; i < 8; ++i) all = all && (gv[i] >> 24) == tag;
    if (__builtin_amdgcn_ballot_w64(!all) == 0) break;
    __builtin_amdgcn_s_sleep(1);
#pragma unroll
    for (int i = 0; i < 8; ++i)
      if ((gv[i] >> 24) != tag) gv[i] = __hip_atomic_load(ib + 64 * i, __ATOMIC_RELAXED, __HIP_MEMORY_SCOPE_AGENT);
  }
}

struct Soft { float m, mc, l; f32x4 acc; };

template <bool ISSUE>
__device__ __forceinline__ void tile_step(v4u (&kr)[4], v4u (&vr)[4], const GAS v4u* kn, const GAS v4u* vn, const v4u q,
                                          const unsigned (&mask)[4], bool hi8, bool hi4, Soft& s) {
  float x[4];
#pragma unroll
  for (int t = 0; t < 4; ++t) {
    float d = dot2(kr[t][0], q[0], 0.f);
    d = dot2(kr[t][1], q[1], d); d = dot2(kr[t][2], q[2], d); d = dot2(kr[t][3], q[3], d);
    x[t] = d;
    if (ISSUE) { asm volatile("" : "+v"(x[t])); KV_THROTTLE(); kr[t] = ld_nt(kn + t * 64); SCHED_FENCE(); }
  }
  const float a0 = x[0] + dppf<XOR8>(x[0]), a2 = x[2] + dppf<XOR8>(x[2]);
  const float a1 = x[1] + dppf<XOR8>(x[1]), a3 = x[3] + dppf<XOR8>(x[3]);
  const float y0 = hi8 ? a2 : a0, y1 = hi8 ? a3 : a1;
  const float b0 = y0 + dppf<XOR7>(y0), b1 = y1 + dppf<XOR7>(y1);
  float z = hi4 ? b1 : b0;
  z += dppf<XOR2>(z); z += dppf<XOR1>(z);
  if (__builtin_amdgcn_ballot_w64(z > s.m) != 0) {
    const float mn = fmaxf(s.m, wave_max(z));
    const float mcn = mn * SCL;
    const float alpha = __builtin_amdgcn_exp2f(s.mc - mcn);
    s.m = mn; s.mc = mcn; s.l *= alpha; s.acc *= alpha;
  }
  const float p = __builtin_amdgcn_exp2f(fmaf(z, SCL, -s.mc));
  s.l += p;
  const unsigned pk = pk_bf16(p, p);
#define PV_STEP(t) { const v4u bf = {dppu<BCAST + 4 * t>(pk) & mask[0], dppu<BCAST + 4 * t>(pk) & mask[1], dppu<BCAST + 4 * t>(pk) & mask[2], dppu<BCAST + 4 * t>(pk) & mask[3]}; \
    s.acc = mfma(vr[t], bf, s.acc); if (ISSUE) { SCHED_FENCE(); KV_THROTTLE(); vr[t] = ld_nt(vn + t * 64); SCHED_FENCE(); } }
  PV_STEP(0) PV_STEP(1) PV_STEP(2) PV_STEP(3)
#undef PV_STEP
}

__global__ void __launch_bounds__(NTHR, 3) fused_kernel(Args a_unused) {
  u64 dg_first; asm volatile("s_memrealtime %0\n\ts_waitcnt lgkmcnt(0)" : "=s"(dg_first) :: "memory");
  const __attribute__((address_space(4))) Args* ap = (const __attribute__((address_space(4))) Args*)__builtin_amdgcn_kernarg_segment_ptr();
  asm volatile("" : "+s"(ap));
  Args a; a.x = ap->x; a.k = ap->k; a.v = ap->v; a.wq = ap->wq; a.wo = ap->wo; a.w1 = ap->w1; a.b1 = ap->b1; a.w2 = ap->w2; a.b2 = ap->b2; a.gw = ap->gw; a.out = ap->out; a.ws = ap->ws;
  extern __shared__ __attribute__((aligned(16))) unsigned char smem[];
  LAS unsigned char* L = (LAS unsigned char*)smem;
  const int tid = threadIdx.x, lane = tid & 63, w = __builtin_amdgcn_readfirstlane(tid >> 6);
  const int bid = blockIdx.x;
  const int h = (bid & 7) * 4 + ((bid >> 3) & 3), b = bid >> 5;
  const int g = lane >> 4, c = lane & 15;
  const u64 dg_t0 = dg_first;
  unsigned dg_q = 0, dg_e = 0;

  if (w >= NWA) {
    const int ws_ = w - NWA;
    const size_t kvoff = ((size_t)(b * NH + h) * SK + ws_ * TK) * DH;
    const GAS v4u* kp = (const GAS v4u*)(a.k + kvoff) + lane;
    const GAS v4u* vp = (const GAS v4u*)(a.v + kvoff) + lane;
    constexpr int TSTR = NWS * TK * DH / 8;
    v4u kr[DEPTH][4], vr[DEPTH][4];
    int rot = GM_ROT == 0 ? 0 : GM_ROT == 1 ? (int)((bid * 2654435761u) >> 27) : GM_ROT == 2 ? (int)(((bid * 8 + ws_) * 2654435761u) >> 27) : GM_ROT == 3 ? (bid >> 5) * 4 : GM_ROT == 4 ? ws_ * 4 : (bid >> 3);
#define TOFF(i) ((((i) + rot) & (NT - 1)) * TSTR)
    if (GM_B0) WG_BARRIER();
#pragma unroll
    for (int d = 0; d < DEPTH; ++d) {
#pragma unroll
      for (int t = 0; t < 4; ++t) { KV_THROTTLE0(); kr[d][t] = ld_nt(kp + TOFF(d) + t * 64); SCHED_FENCE(); }
#pragma unroll
      for (int t = 0; t < 4; ++t) { KV_THROTTLE0(); vr[d][t] = ld_nt(vp + TOFF(d) + t * 64); SCHED_FENCE(); }
    }
    unsigned mask[4];
#pragma unroll
    for (int r = 0; r < 4; ++r) mask[r] = (c >> 1) == r ? ((c & 1) ? 0xffff0000u : 0x0000ffffu) : 0u;
    LAS float* part = (LAS float*)(L + L_PART);
    WG_BARRIER();
    const v4u q = *(const LAS v4u*)(L + L_Q + 16 * c);
    dg_q = (unsigned)(__builtin_amdgcn_s_memrealtime() - dg_t0);
    Soft s; s.m = -INFINITY; s.mc = -INFINITY; s.l = 0.f; s.acc = f32x4{0.f, 0.f, 0.f, 0.f};
    const bool hi8 = (c & 8) != 0, hi4 = (c & 4) != 0;
    constexpr int REM = NT % DEPTH;
#pragma unroll 1
    for (int it = 0; it < NT / DEPTH - 1; ++it) {
      rot += DEPTH;
#pragma unroll
      for (int d = 0; d < DEPTH; ++d) tile_step<true>(kr[d], vr[d], kp + TOFF(d), vp + TOFF(d), q, mask, hi8, hi4, s);
    }
    rot += DEPTH;
#pragma unroll
    for (int d = 0; d < DEPTH; ++d) {
      if (d < REM) tile_step<true>(kr[d], vr[d], kp + TOFF(d), vp + TOFF(d), q, mask, hi8, hi4, s);
      else tile_step<false>(kr[d], vr[d], kp, vp, q, mask, hi8, hi4, s);
    }
#pragma unroll
    for (int d = 0; d < REM; ++d) tile_step<false>(kr[d], vr[d], kp, vp, q, mask, hi8, hi4, s);
    const float lsum = wave_sum(s.l) * 0.25f;
    if (c < 8) {
#pragma unroll
      for (int r = 0; r < 4; ++r) part[ws_ * DH + 8 * (4 * g + r) + c] = s.acc[r];
    }
    if (lane == 0) { ((LAS float*)(L + L_ML))[2 * ws_] = s.mc; ((LAS float*)(L + L_ML))[2 * ws_ + 1] = lsum; }
  } else if (w > 0) {
    const int m = w - 1;
    if (GM_PRIO) __builtin_amdgcn_s_setprio(3);
    if (GM_B0) WG_BARRIER();
    WG_BARRIER();
    const GAS u16* xp = (const GAS u16*)(a.x + b * DM);
    float xv[9];
#pragma unroll
    for (int i = 0; i < 9; ++i) xv[i] = bf2f(xp[lane + 64 * i]);
    const int col0 = 64 * ((9 * h) / 4 + m), sl = 4 * (c >> 3) + g;
    const GAS unsigned char* w1p = (const GAS unsigned char*)a.w1 + (size_t)(72 * sl) * (FF * 2) + col0 * 2 + (c & 7) * 16;
    constexpr int R1 = GM_R1;
    v4u r1[R1];
#pragma unroll
    for (int i = 0; i < R1; ++i) { r1[i] = ld_w((const GAS v4u*)(w1p + i * (FF * 2))); SCHED_FENCE(); }
    float b1v[4];
#pragma unroll
    for (int r = 0; r < 4; ++r) b1v[r] = bf2f(((const GAS u16*)a.b1)[col0 + 8 * (4 * (g & 1) + r) + (c & 7)]);
    if (m == 0) {
      const GAS u16* gp = (const GAS u16*)a.gw;
      float gs = 0.f;
#pragma unroll
      for (int i = 0; i < 9; ++i) gs += rbf(xv[i] * bf2f(gp[lane + 64 * i]));
      const float logit = rbf(wave_sum(gs));
      const float gate = rbf(1.f / (1.f + __expf(-logit)));
      if (lane == 0) *(LAS float*)(L + L_MISC) = gate;
    }
    float sum = 0.f;
#pragma unroll
    for (int i = 0; i < 9; ++i) sum += xv[i];
    const float mean = wave_sum(sum) * (1.f / DM);
    float sq = 0.f;
#pragma unroll
    for (int i = 0; i < 9; ++i) sq += (xv[i] - mean) * (xv[i] - mean);
    const float var = rbf(wave_sum(sq) * (1.f / DM));
    const float mb = rbf(mean), sd = rbf(sqrtf(rbf(var + 1e-5f)));
    LAS u16* yl = (LAS u16*)(L + L_Y + m * 1152);
#pragma unroll
    for (int i = 0; i < 9; ++i) yl[lane + 64 * i] = __builtin_bit_cast(u16, (__bf16)(rbf(xv[i] - mb) / sd));
    asm volatile("s_waitcnt lgkmcnt(0)" ::: "memory");
    unsigned mask[4];
#pragma unroll
    for (int r = 0; r < 4; ++r) mask[r] = ((c & 7) >> 1) == r ? ((c & 1) ? 0xffff0000u : 0x0000ffffu) : 0u;
    f32x4 h1 = {0.f, 0.f, 0.f, 0.f};
    static_assert(24 % R1 == 0, "ring");
#pragma unroll 1
    for (int o = 0; o < 72; o += 24) {
      v4u yy[3];
#pragma unroll
      for (int i = 0; i < 3; ++i) yy[i] = *(const LAS v4u*)(yl + 72 * sl + o + 8 * i);
#pragma unroll
      for (int i = 0; i < 24; ++i) {
        const unsigned u = yy[i >> 3][(i >> 1) & 3];
        const unsigned dup = __builtin_amdgcn_perm(u, u, (i & 1) ? 0x03020302u : 0x01000100u);
        h1 = mfma(r1[i % R1], v4u{dup & mask[0], dup & mask[1], dup & mask[2], dup & mask[3]}, h1);
        SCHED_FENCE();
        const int nx = o + i + R1 < 72 ? o + i + R1 : 71;
        r1[i % R1] = ld_w((const GAS v4u*)(w1p + (size_t)nx * (FF * 2)));
        SCHED_FENCE();
      }
    }
    const int lo = col0 > FS * h ? col0 : FS * h, hi = col0 + 64 < FS * h + FS ? col0 + 64 : FS * h + FS;
    const int nrg = (hi - lo) >> 3;
    const GAS unsigned char* w2p = (const GAS unsigned char*)a.w2 + (size_t)(lo + sl) * (DM * 2) + (c & 7) * 16;
    v4u r2[3];
#pragma unroll
    for (int i = 0; i < 3; ++i) { r2[i] = ld_w((const GAS v4u*)(w2p + i * 128)); SCHED_FENCE(); }
    LAS u16* hid = (LAS u16*)(L + L_HID + m * 128);
#pragma unroll
    for (int r = 0; r < 4; ++r) {
      const float pre = rbf(rbf(fold_halves(h1[r])) + b1v[r]);
      const float inner = 0.7978845608028654f * (pre + 0.044715f * pre * pre * pre);
      const float th = 1.f - 2.f / (1.f + __expf(2.f * inner));
      if (lane < 32 && c < 8) hid[8 * (4 * g + r) + c] = __builtin_bit_cast(u16, (__bf16)(0.5f * pre * (1.f + th)));
    }
    asm volatile("s_waitcnt lgkmcnt(0)" ::: "memory");
    f32x4 o2[9];
#pragma unroll
    for (int j = 0; j < 9; ++j) o2[j] = f32x4{0.f, 0.f, 0.f, 0.f};
#pragma unroll 1
    for (int rg = 0; rg < nrg; ++rg) {
      const unsigned u = hid[lo - col0 + 8 * rg + sl];
      const unsigned dup = u | (u << 16);
      const v4u bf = {dup & mask[0], dup & mask[1], dup & mask[2], dup & mask[3]};
      const GAS unsigned char* cur = w2p + (size_t)rg * (8 * DM * 2);
      const GAS unsigned char* nxt = w2p + (size_t)(rg + 1 < nrg ? rg + 1 : rg) * (8 * DM * 2);
#pragma unroll
      for (int j = 0; j < 9; ++j) {
        o2[j] = mfma(r2[j % 3], bf, o2[j]);
        SCHED_FENCE();
        r2[j % 3] = ld_w((const GAS v4u*)(j < 6 ? cur + (j + 3) * 128 : nxt + (j - 6) * 128));
        SCHED_FENCE();
      }
    }
    LAS float* mp = (LAS float*)(L + L_MLP + m * 2304);
#pragma unroll
    for (int j = 0; j < 9; ++j)
#pragma unroll
      for (int r = 0; r < 4; ++r) {
        const float t = fold_halves(o2[j][r]);
        if (lane < 32 && c < 8) mp[64 * j + 8 * (4 * g + r) + c] = t;
      }
  } else {
    if (GM_PRIO) __builtin_amdgcn_s_setprio(3);
    {
      u64 e1 = *(const __attribute__((address_space(4))) u64*)(a.ws + W_EPOCH);
      const int ch = b & 1, kq = b >> 1;
      const GAS unsigned char* wqb = (const GAS unsigned char*)a.wq + (size_t)(144 * kq + 8 * (lane >> 5) + ((lane >> 3) & 3)) * (NH * DH * 2)
                                     + h * 256 + ch * 128 + (((((lane & 7) >> 1) ^ g) << 1) | (lane & 1)) * 16;
#pragma unroll
      for (int i = 0; i < 18; ++i) glds16(wqb + (size_t)(16 * (i >> 1) + 4 * (i & 1)) * (NH * DH * 2), (unsigned)(size_t)(L + L_WO) + i * 1024);
      const GAS unsigned char* xb = (const GAS unsigned char*)a.x + (c & 7) * (DM * 2) + 288 * kq;
      v4u xa[5];
#pragma unroll
      for (int ks = 0; ks < 4; ++ks) xa[ks] = *(const GAS v4u*)(xb + 64 * ks + 16 * g);
      xa[4] = *(const GAS v4u*)(xb + 256 + 16 * (g & 1));
      SCHED_FENCE();
      if (GM_B0) asm volatile("s_barrier" ::: "memory");
      asm volatile("s_waitcnt vmcnt(0)" : "+v"(xa[0]), "+v"(xa[1]), "+v"(xa[2]), "+v"(xa[3]), "+v"(xa[4]) :: "memory");
      if (g >= 2) xa[4] = v4u{0u, 0u, 0u, 0u};
      asm volatile("" : "+s"(e1));
      e1 += 1;
      const u64 tag = e1 & ((1ull << 40) - 1);
      if (lane == 0) *(LAS u64*)(L + L_MISC + 8) = e1;
      const int q4 = (lane >> 2) & 3, p4 = lane & 3, sw = 2 * (g & 1) + (q4 >> 1);
      const int rb = (4 * (g & 1) + q4) * 128 + 8 * p4;
      f32x4 qc[4];
#pragma unroll
      for (int cb = 0; cb < 4; ++cb) {
        qc[cb] = f32x4{0.f, 0.f, 0.f, 0.f};
        LAS unsigned char* tb = L + L_WO + rb + ((cb ^ sw) * 32);
#pragma unroll
        for (int ks = 0; ks < 5; ++ks) {
          LAS unsigned char* t2 = tb + ks * 4096 + (ks < 4 ? (g >> 1) * 2048 : 0);
          const v2u l2 = __builtin_bit_cast(v2u, __builtin_amdgcn_ds_read_tr16_b64_v4i16((LAS s16x4*)t2));
          const v2u h2 = __builtin_bit_cast(v2u, __builtin_amdgcn_ds_read_tr16_b64_v4i16((LAS s16x4*)(t2 + 1024)));
          qc[cb] = mfma(xa[ks], v4u{l2[0], l2[1], h2[0], h2[1]}, qc[cb]);
        }
      }
      GAS u64* qx = (GAS u64*)(a.ws + W_QX);
#pragma unroll
      for (int pr = 0; pr < 2; ++pr)
#pragma unroll
        for (int r = 0; r < 4; ++r) {
          const float val = g < 2 ? qc[2 * pr][r] : qc[2 * pr + 1][r];
          const u64 gr = (tag << 24) | (u64)((__builtin_bit_cast(unsigned, val) + 0x80u) >> 8);
          __hip_atomic_store(qx + ((size_t)(h * NB + 4 * (g & 1) + r) * 4 + kq) * 128 + 64 * ch + 16 * (2 * pr + (g >> 1)) + c, gr, __ATOMIC_RELAXED, __HIP_MEMORY_SCOPE_AGENT);
        }
      GAS u64* ib = qx + (size_t)(h * NB + b) * 512 + lane;
      u64 gv[8];
      gather8(ib, tag, gv);
      const float q0 = (GVAL(gv[0]) + GVAL(gv[2])) + (GVAL(gv[4]) + GVAL(gv[6]));
      const float q1 = (GVAL(gv[1]) + GVAL(gv[3])) + (GVAL(gv[5]) + GVAL(gv[7]));
      ((LAS u16*)(L + L_Q))[lane] = __builtin_bit_cast(u16, (__bf16)q0);
      ((LAS u16*)(L + L_Q))[64 + lane] = __builtin_bit_cast(u16, (__bf16)q1);
    }
    WG_BARRIER();
    const GAS unsigned char* wob = (const GAS unsigned char*)(a.wo + (size_t)h * DH * DM);
    int so[9];
#pragma unroll
    for (int j = 0; j < 9; ++j) {
      const int sl = 64 * j + lane, row = sl / 72, ch = sl % 72;
      so[j] = row * 1152 + ((((ch >> 1) ^ ((row >> 1) & 3)) << 1) | (ch & 1)) * 16;
    }
#pragma unroll 1
    for (int gr = 0; gr < 16; ++gr) {
#pragma unroll
      for (int j = 0; j < 9; ++j)
      { glds16(wob + gr * 9216 + so[j], (unsigned)(size_t)(L + L_WO) + gr * 9216 + j * 1024); AUX_THROTTLE(); }
    }
    asm volatile("s_waitcnt vmcnt(0)" ::: "memory");
  }
  STAMP(2);
  WG_BARRIER();
  dg_e = (unsigned)(__builtin_amdgcn_s_memrealtime() - dg_t0);
  STAMP(3);
  if (tid < DH) {
    const LAS float* ml = (const LAS float*)(L + L_ML);
    const LAS float* part = (const LAS float*)(L + L_PART);
    float mx = ml[0];
#pragma unroll
    for (int i = 1; i < NWS; ++i) mx = fmaxf(mx, ml[2 * i]);
    float den = 0.f, num = 0.f;
#pragma unroll
    for (int i = 0; i < NWS; ++i) {
      const float f = __builtin_amdgcn_exp2f(ml[2 * i] - mx);
      den = fmaf(f, ml[2 * i + 1], den); num = fmaf(f, part[i * DH + tid], num);
    }
    const int pos = (tid & 96) + 8 * ((tid >> 2) & 3) + 4 * ((tid >> 4) & 1) + (tid & 3);
    ((LAS u16*)(L + L_OBF))[pos] = __builtin_bit_cast(u16, (__bf16)(num / den));
  }
  WG_BARRIER();
  const float gate = *(const LAS float*)(L + L_MISC);
  const float omg = rbf(1.f - gate);
  const u64 ep1 = *(const LAS u64*)(L + L_MISC + 8);
  const u64 tag = ep1 & ((1ull << 40) - 1);
  {
    const int q4 = (lane >> 2) & 3, p4 = lane & 3;
    const int base = (4 * g + q4) * 1152 + ((w ^ ((2 * g + (q4 >> 1)) & 3)) * 32) + 8 * p4;
    f32x4 oc[3];
#pragma unroll
    for (int j = 0; j < 3; ++j) oc[j] = f32x4{0.f, 0.f, 0.f, 0.f};
#pragma unroll
    for (int ks = 0; ks < 4; ++ks) {
      const v4u af = *(const LAS v4u*)(L + L_OBF + 64 * ks + 16 * g);
#pragma unroll
      for (int j = 0; j < 3; ++j) {
        const s16x4 lo = __builtin_amdgcn_ds_read_tr16_b64_v4i16((LAS s16x4*)(L + L_WO + base + ks * 32 * 1152 + j * 384));
        const s16x4 hi = __builtin_amdgcn_ds_read_tr16_b64_v4i16((LAS s16x4*)(L + L_WO + base + (ks * 32 + 16) * 1152 + j * 384));
        const v2u l2 = __builtin_bit_cast(v2u, lo), h2 = __builtin_bit_cast(v2u, hi);
        oc[j] = mfma(af, v4u{l2[0], l2[1], h2[0], h2[1]}, oc[j]);
      }
    }
    if (lane < 48) {
      const int cb = w + 12 * g, n = 16 * cb + c;
      const float av = g == 0 ? oc[0][0] : (g == 1 ? oc[1][0] : oc[2][0]);
      const LAS float* mp = (const LAS float*)(L + L_MLP);
      const float tot = gate * av + omg * ((mp[n] + mp[DM + n]) + mp[2 * DM + n]);
      const u64 gr = (tag << 24) | (u64)((__builtin_bit_cast(unsigned, tot) + 0x80u) >> 8);
      __hip_atomic_store((GAS u64*)(a.ws + W_INBOX) + ((size_t)(b * 36 + cb) * 32 + h) * 16 + c, gr, __ATOMIC_RELAXED, __HIP_MEMORY_SCOPE_AGENT);
    }
  }
  STAMP(4);
  if (w == 0 || (w == 1 && h < 4)) {
    const int cb = h + 32 * w;
    const float b2v = bf2f(((const GAS u16*)a.b2)[16 * cb + c]);
    GAS u64* ib = (GAS u64*)(a.ws + W_INBOX) + (size_t)(b * 36 + cb) * 32 * 16 + lane;
    u64 gv[8];
    gather8(ib, tag, gv);
    float sum = 0.f;
#pragma unroll
    for (int i = 0; i < 8; ++i) sum += __builtin_bit_cast(float, (unsigned)gv[i] << 8);
    sum = xrow_sum(sum);
    if (lane < 16) ((GAS u16*)a.out)[b * DM + 16 * cb + c] = __builtin_bit_cast(u16, (__bf16)(sum + omg * b2v));
    if (w == 0 && bid == 0 && lane == 0)
      __hip_atomic_store((GAS u64*)(a.ws + W_EPOCH), ep1, __ATOMIC_RELAXED, __HIP_MEMORY_SCOPE_AGENT);
    STAMP(5);
  }
  if (w == 4) {
    unsigned metric = 0;
    if (DG_MODE == 1) metric = 10 * dg_q;
    if (DG_MODE == 2) metric = 4 * dg_e;
    if (DG_MODE == 3) metric = 40000 - 4 * dg_e;
    if (DG_MODE == 4) metric = 1000 * __builtin_ctzll((u64)a.k) ;
    if (DG_MODE == 5) metric = 1000 * __builtin_ctzll((u64)a.v - (u64)a.k);
    const unsigned tgt = 15000 + metric;
    while ((unsigned)(__builtin_amdgcn_s_memrealtime() - dg_t0) < tgt) __builtin_amdgcn_s_sleep(2);
  }
}

}

extern "C" void kernel_launch(void* const* d_in, const int* in_sizes, int n_in, void* d_out, int out_size, void* d_ws,
                              size_t ws_size, hipStream_t stream) {
  (void)in_sizes; (void)n_in; (void)out_size; (void)ws_size;
  gm::Args a;
  a.x = (const gm::u16*)d_in[0]; a.k = (const gm::u16*)d_in[1]; a.v = (const gm::u16*)d_in[2];
  a.wq = (const gm::u16*)d_in[3]; a.wo = (const gm::u16*)d_in[4];
  a.w1 = (const gm::u16*)d_in[6]; a.b1 = (const gm::u16*)d_in[7]; a.w2 = (const gm::u16*)d_in[8]; a.b2 = (const gm::u16*)d_in[9];
  a.gw = (const gm::u16*)d_in[10];
  a.out = (gm::u16*)d_out; a.ws = (unsigned char*)d_ws;
  (void)hipFuncSetAttribute((const void*)gm::fused_kernel, hipFuncAttributeMaxDynamicSharedMemorySize, gm::L_BYTES);
  hipLaunchKernelGGL(gm::fused_kernel, dim3(gm::NB * gm::NH), dim3(gm::NTHR), gm::L_BYTES, stream, a);
}
```

```cpp
#include <hip/hip_runtime.h>
#include <hip/hip_bf16.h>
#include <stdint.h>

namespace gm {

constexpr int NB = 8, NH = 32, SK = 4096, DH = 128, DM = 576, FF = 4608;
constexpr int FS = FF / NH;
constexpr int NWS = 4, NWA = 4, NTHR = (NWS + NWA) * 64;
constexpr int TK = 16;
constexpr int NT = SK / TK / NWS;
#define GM_DEPTH 2
constexpr int DEPTH = GM_DEPTH;
#define GM_EXTRA 2
constexpr int EXTRA = GM_EXTRA;
static_assert((NT - EXTRA) % DEPTH == 0, "whole rounds");
#define GM_SW 5
#define GM_SW0 GM_SW
#define GM_AW 2
#define GM_R1 3
#define GM_FREE 0
#define GM_TOUCH 0
#define GM_QSLEEP 40
#define GM_PAT 0
#define GM_LO 3
#define GM_HI 7
#define GM_BAND 30
#define GM_P0 1808
#define GM_CTL 2
#define GM_ROT 1
#define GM_PRED 5
#define GM_CSLEEP 40
#define GM_OFF 130
#define GM_P1 1815
#define GM_P2 1750
#define GM_I1 52
#define GM_STEP 75
#define GM_TA 600
#define GM_AD 5400
#define STR2(x) #x
#define STR(x) STR2(x)
#define VMWAIT(n) asm volatile("s_waitcnt vmcnt(" STR(n) ")" ::: "memory")

constexpr int L_WO = 0;
constexpr int A_STR = 272;
constexpr int L_A = 128 * 1152;
constexpr int L_MLP = L_A + 8 * A_STR;
constexpr int L_Y = L_MLP + 3 * 2304;
constexpr int L_HID = L_Y + 3 * 1152;
constexpr int L_ML = L_HID + 3 * 128;
constexpr int L_Q = L_ML + 32;
constexpr int L_MISC = L_Q + 256;
constexpr int L_BYTES = L_MISC + 16;
static_assert(L_BYTES <= 163840 && L_MLP % 16 == 0 && L_ML % 16 == 0 && L_MISC % 16 == 0, "LDS");

constexpr size_t W_EPOCH = 0;
constexpr size_t W_PACE = 1024;
constexpr size_t W_QX = 4096;
constexpr size_t W_OX = W_QX + (size_t)NH * NB * 4 * 128 * 8;

#define GAS __attribute__((address_space(1)))
#define LAS __attribute__((address_space(3)))
#define CAS __attribute__((address_space(4)))
typedef unsigned v4u __attribute__((ext_vector_type(4)));
typedef unsigned v2u __attribute__((ext_vector_type(2)));
typedef float f32x4 __attribute__((ext_vector_type(4)));
typedef short s16x4 __attribute__((ext_vector_type(4)));
typedef __bf16 bf16x2 __attribute__((ext_vector_type(2)));
typedef __bf16 bf16x8 __attribute__((ext_vector_type(8)));
typedef unsigned long long u64;
typedef unsigned short u16;
typedef __amdgpu_buffer_rsrc_t rsrc_t;

struct Args {
  const u16* k; const u16* v; const u16* wq; const u16* x; unsigned char* ws;
  const u16* wo; const u16* w1; const u16* b1; const u16* w2; const u16* b2; const u16* gw; u16* out;
};

__device__ __forceinline__ v4u ld_nt(const GAS v4u* p) { return __builtin_nontemporal_load(p); }
__device__ __forceinline__ v4u ld_kv(rsrc_t rs, int voff, int soff) { return __builtin_amdgcn_raw_buffer_load_b128(rs, voff, soff, 2); }
__device__ __forceinline__ void glds16(const GAS unsigned char* g, unsigned lds_base) {
  unsigned keep;
  asm volatile("s_mov_b32 %0, m0\n\ts_mov_b32 m0, %2\n\ts_nop 0\n\tglobal_load_lds_dwordx4 %1, off nt\n\ts_mov_b32 m0, %0" : "=&s"(keep) : "v"(g), "s"(lds_base) : "memory");
}
__device__ __forceinline__ float bf2f(unsigned h) { return __builtin_bit_cast(float, h << 16); }
__device__ __forceinline__ float bflo(unsigned u) { return __builtin_bit_cast(float, u << 16); }
__device__ __forceinline__ float bfhi(unsigned u) { return __builtin_bit_cast(float, u & 0xffff0000u); }
__device__ __forceinline__ unsigned pk_bf16(float lo, float hi) {
  bf16x2 p = {(__bf16)lo, (__bf16)hi};
  return __builtin_bit_cast(unsigned, p);
}
__device__ __forceinline__ float rbf(float f) { return (float)(__bf16)f; }
__device__ __forceinline__ u16 f2bf(float f) { return __builtin_bit_cast(u16, (__bf16)f); }
__device__ __forceinline__ float dot2(unsigned a, unsigned b, float c) {
  return __builtin_amdgcn_fdot2_f32_bf16(__builtin_bit_cast(bf16x2, a), __builtin_bit_cast(bf16x2, b), c, false);
}
__device__ __forceinline__ f32x4 mfma(v4u a, v4u b, f32x4 c) {
  return __builtin_amdgcn_mfma_f32_16x16x32_bf16(__builtin_bit_cast(bf16x8, a), __builtin_bit_cast(bf16x8, b), c, 0, 0, 0);
}
__device__ __forceinline__ v4u tr_frag(LAS unsigned char* lo, LAS unsigned char* hi) {
  const v2u l2 = __builtin_bit_cast(v2u, __builtin_amdgcn_ds_read_tr16_b64_v4i16((LAS s16x4*)lo));
  const v2u h2 = __builtin_bit_cast(v2u, __builtin_amdgcn_ds_read_tr16_b64_v4i16((LAS s16x4*)hi));
  return v4u{l2[0], l2[1], h2[0], h2[1]};
}
template <int CTRL> __device__ __forceinline__ float dppf(float x) {
  return __builtin_bit_cast(float, __builtin_amdgcn_mov_dpp(__builtin_bit_cast(int, x), CTRL, 0xf, 0xf, true));
}
template <int CTRL> __device__ __forceinline__ unsigned dppu(unsigned x) {
  return (unsigned)__builtin_amdgcn_mov_dpp((int)x, CTRL, 0xf, 0xf, true);
}
constexpr int XOR1 = 0xB1, XOR2 = 0x4E, XOR7 = 0x141, XOR8 = 0x128, BCAST = 0x150;
__device__ __forceinline__ float x16_sum(float x) {
  auto s = __builtin_amdgcn_permlane16_swap(__float_as_uint(x), __float_as_uint(x), false, false);
  return __uint_as_float(s[0]) + __uint_as_float(s[1]);
}
__device__ __forceinline__ float xrow_sum(float x) {
  x = x16_sum(x);
  auto t = __builtin_amdgcn_permlane32_swap(__float_as_uint(x), __float_as_uint(x), false, false);
  return __uint_as_float(t[0]) + __uint_as_float(t[1]);
}
__device__ __forceinline__ float xrow_max(float x) {
  auto s = __builtin_amdgcn_permlane16_swap(__float_as_uint(x), __float_as_uint(x), false, false);
  x = fmaxf(__uint_as_float(s[0]), __uint_as_float(s[1]));
  auto t = __builtin_amdgcn_permlane32_swap(__float_as_uint(x), __float_as_uint(x), false, false);
  return fmaxf(__uint_as_float(t[0]), __uint_as_float(t[1]));
}
__device__ __forceinline__ float fold_halves(float x) {
  const float t = dppf<XOR8>(x);
  auto p = __builtin_amdgcn_permlane32_swap(__float_as_uint(t), __float_as_uint(t), false, false);
  return x + __uint_as_float(p[1]);
}
__device__ __forceinline__ float wave_sum(float x) {
  x += dppf<XOR1>(x); x += dppf<XOR2>(x); x += dppf<XOR7>(x); x += dppf<XOR8>(x);
  return xrow_sum(x);
}
__device__ __forceinline__ float wave_max(float x) {
  x = fmaxf(x, dppf<XOR1>(x)); x = fmaxf(x, dppf<XOR2>(x)); x = fmaxf(x, dppf<XOR7>(x)); x = fmaxf(x, dppf<XOR8>(x));
  return xrow_max(x);
}
#define WG_BARRIER() asm volatile("s_waitcnt lgkmcnt(0)\n\ts_barrier" ::: "memory")
#define LDS_WAIT() asm volatile("s_waitcnt lgkmcnt(0)" ::: "memory")
#define SCHED_FENCE() __builtin_amdgcn_sched_barrier(0)
#define RLX_AGENT __ATOMIC_RELAXED, __HIP_MEMORY_SCOPE_AGENT

constexpr float SCL = 0.088388347648318440f * 1.4426950408889634f;
constexpr u64 TAG_MASK = (1ull << 40) - 1;

__device__ __forceinline__ u64 granule(u64 tag, float v) { return (tag << 24) | (u64)((__builtin_bit_cast(unsigned, v) + 0x80u) >> 8); }
#define GVAL(x) __builtin_bit_cast(float, (unsigned)(x) << 8)
__device__ __forceinline__ void gather8(GAS u64* ib, u64 tag, u64 (&gv)[8]) {
#pragma unroll
  for (int i = 0; i < 8; ++i) gv[i] = __hip_atomic_load(ib + 64 * i, RLX_AGENT);
  for (unsigned spin = 0; spin < (1u << 22); ++spin) {
    bool all = true;
#pragma unroll
    for (int i = 0; i < 8; ++i) all = all && (gv[i] >> 24) == tag;
    if (__builtin_amdgcn_ballot_w64(!all) == 0) break;
    __builtin_amdgcn_s_sleep(1);
#pragma unroll
    for (int i = 0; i < 8; ++i)
      if ((gv[i] >> 24) != tag) gv[i] = __hip_atomic_load(ib + 64 * i, RLX_AGENT);
  }
}

struct Soft { float m, mc, l; f32x4 acc; };
constexpr int LAG_FREE = 1 << 20;
#define THROTTLE(lag, i) asm volatile( \
    "s_cmp_ge_i32 %0, 0x100000\n\ts_cbranch_scc1 1f\n\ts_waitcnt vmcnt(%c1)\n\t" \
    "s_cmp_gt_i32 %0, %c5\n\ts_cbranch_scc1 1f\n\ts_waitcnt vmcnt(%c2)\n\t" \
    "s_cmp_gt_i32 %0, %c6\n\ts_cbranch_scc1 1f\n\ts_waitcnt vmcnt(%c3)\n\t" \
    "s_cmp_gt_i32 %0, %c7\n\ts_cbranch_scc1 1f\n\ts_waitcnt vmcnt(%c4)\n1:" \
    :: "s"(lag), "i"(GM_LO + 3), "i"(GM_LO + 2), "i"(GM_LO + 1), "i"(GM_LO), \
       "i"(GM_STEP + DITH(i)), "i"(DITH(i)), "i"(-GM_STEP + DITH(i)) : "memory", "scc")
#define DITH(i) (GM_STEP * ((((i) & 1) * 4 + ((i) & 2) + ((i) >> 2)) * 2 + 1) / 16)

template <bool ISSUE>
__device__ __forceinline__ void tile_step(v4u (&kr)[4], v4u (&vr)[4], rsrc_t rk, rsrc_t rv, int voff, int soff, int lvl, const v4u q,
                                          const unsigned (&mask)[4], bool hi8, bool hi4, Soft& s) {
  float x[4];
#pragma unroll
  for (int t = 0; t < 4; ++t) {
    float d = dot2(kr[t][0], q[0], 0.f);
    d = dot2(kr[t][1], q[1], d); d = dot2(kr[t][2], q[2], d); d = dot2(kr[t][3], q[3], d);
    x[t] = d;
    if (ISSUE) {
      asm volatile("" : "+v"(x[t]));
      if (t == 0) THROTTLE(lvl, 0); else if (t == 1) THROTTLE(lvl, 1); else if (t == 2) THROTTLE(lvl, 2); else THROTTLE(lvl, 3);
      kr[t] = ld_kv(rk, voff + t * 1024, soff);
      SCHED_FENCE();
    }
  }
  const float a0 = x[0] + dppf<XOR8>(x[0]), a2 = x[2] + dppf<XOR8>(x[2]);
  const float a1 = x[1] + dppf<XOR8>(x[1]), a3 = x[3] + dppf<XOR8>(x[3]);
  const float y0 = hi8 ? a2 : a0, y1 = hi8 ? a3 : a1;
  const float b0 = y0 + dppf<XOR7>(y0), b1 = y1 + dppf<XOR7>(y1);
  float z = hi4 ? b1 : b0;
  z += dppf<XOR2>(z); z += dppf<XOR1>(z);
  if (__builtin_amdgcn_ballot_w64(z > s.m) != 0) {
    const float mn = fmaxf(s.m, wave_max(z));
    const float mcn = mn * SCL;
    const float alpha = __builtin_amdgcn_exp2f(s.mc - mcn);
    s.m = mn; s.mc = mcn; s.l *= alpha; s.acc *= alpha;
  }
  const float p = __builtin_amdgcn_exp2f(fmaf(z, SCL, -s.mc));
  s.l += p;
  const unsigned pk = pk_bf16(p, p);
#define PV_STEP(t) { const v4u bf = {dppu<BCAST + 4 * t>(pk) & mask[0], dppu<BCAST + 4 * t>(pk) & mask[1], dppu<BCAST + 4 * t>(pk) & mask[2], dppu<BCAST + 4 * t>(pk) & mask[3]}; \
    s.acc = mfma(vr[t], bf, s.acc); if (ISSUE) { SCHED_FENCE(); THROTTLE(lvl, 4 + t); vr[t] = ld_kv(rv, voff + t * 1024, soff); SCHED_FENCE(); } }
  PV_STEP(0) PV_STEP(1) PV_STEP(2) PV_STEP(3)
#undef PV_STEP
}

#define WAVE_OFF(ws) ((ws) * 4096)
#define TILE_OFF(i) (((i) ^ rot) * 16384)

__global__ void __launch_bounds__(NTHR, 2) fused_kernel(Args a) {
  extern __shared__ __attribute__((aligned(16))) unsigned char smem[];
  LAS unsigned char* L = (LAS unsigned char*)smem;
  const int tid = threadIdx.x, lane = tid & 63, w = __builtin_amdgcn_readfirstlane(tid >> 6);
  const int bid = blockIdx.x;
  const int h = (bid & 7) * 4 + ((bid >> 3) & 3), b = bid >> 5;
  const int g = lane >> 4, c = lane & 15;

  if (__builtin_expect(w >= NWA, 1)) {
    const int ws = w - NWA;
    const unsigned t_zero = (unsigned)__builtin_amdgcn_s_memrealtime();
    const size_t kvoff = (size_t)(b * NH + h) * SK * DH;
    const rsrc_t rk = __builtin_amdgcn_make_buffer_rsrc((void*)(a.k + kvoff), 0, SK * DH * 2, 0x00020000);
    const rsrc_t rv = __builtin_amdgcn_make_buffer_rsrc((void*)(a.v + kvoff), 0, SK * DH * 2, 0x00020000);
    const int voff = lane * 16 + WAVE_OFF(ws);
    const int rot = GM_ROT == 0 ? 0 : GM_ROT == 1 ? ((bid >> 3) & 7) : GM_ROT == 2 ? ((bid >> 3) & 3) : ((bid >> 3) & 1);
    v4u kr[DEPTH][4], vr[DEPTH][4], kx[EXTRA ? EXTRA : 1][4], vx[EXTRA ? EXTRA : 1][4];
#define FINE(i)
#pragma unroll
    for (int d = 0; d < DEPTH + EXTRA; ++d) {
#pragma unroll
      for (int t = 0; t < 4; ++t) {
        VMWAIT(GM_SW0); if (t % 2 == 0) FINE(d * 4 + t / 2);
        const v4u t_ = ld_kv(rk, voff + t * 1024, TILE_OFF(d));
        if (d < DEPTH) kr[d][t] = t_; else kx[d - DEPTH][t] = t_;
        SCHED_FENCE();
      }
#pragma unroll
      for (int t = 0; t < 4; ++t) {
        VMWAIT(GM_SW0); if (t % 2 == 0) FINE(d * 4 + 2 + t / 2);
        const v4u t_ = ld_kv(rv, voff + t * 1024, TILE_OFF(d));
        if (d < DEPTH) vr[d][t] = t_; else vx[d - DEPTH][t] = t_;
        SCHED_FENCE();
      }
    }
    unsigned mask[4];
#pragma unroll
    for (int r = 0; r < 4; ++r) mask[r] = (c >> 1) == r ? ((c & 1) ? 0xffff0000u : 0x0000ffffu) : 0u;
    const bool hi8 = (c & 8) != 0, hi4 = (c & 4) != 0;
    WG_BARRIER();
    const v4u q = *(const LAS v4u*)(L + L_Q + 16 * c);
    Soft s; s.m = -INFINITY; s.mc = -INFINITY; s.l = 0.f; s.acc = f32x4{0.f, 0.f, 0.f, 0.f};
    int nxt = DEPTH + EXTRA;
#pragma unroll
    for (int e = 0; e < EXTRA; ++e) {
#pragma unroll
      for (int d = 0; d < DEPTH; ++d) tile_step<true>(kr[d], vr[d], rk, rv, voff, TILE_OFF(nxt + d), 0, q, mask, hi8, hi4, s);
      nxt += DEPTH;
      tile_step<false>(kx[e], vx[e], rk, rv, voff, 0, 3, q, mask, hi8, hi4, s);
      FINE(30 + ((nxt - EXTRA) >> 1));
    }
#pragma unroll 1
    for (; nxt < NT + DEPTH; nxt += DEPTH) {
#pragma unroll
      for (int d = 0; d < DEPTH; ++d) {
        int lvl = 0;
        if (GM_CTL == 2) {
          const int i_ = nxt + d;
          lvl = (int)((unsigned)__builtin_amdgcn_s_memrealtime() - t_zero) - GM_OFF - ((GM_P2 * i_ + (GM_P1 - GM_P2) * (i_ < GM_I1 ? i_ : GM_I1)) >> 4);
        }
        lvl = nxt + d >= NT - GM_FREE ? LAG_FREE : lvl;
        tile_step<true>(kr[d], vr[d], rk, rv, voff, TILE_OFF(nxt + d), lvl, q, mask, hi8, hi4, s);
      }
      FINE(30 + ((nxt + DEPTH - EXTRA) >> 1));
    }
    const float lsum = wave_sum(s.l) * 0.25f;
    if (c < 8) {
      const unsigned h0 = pk_bf16(s.acc[0], s.acc[1]), h1 = pk_bf16(s.acc[2], s.acc[3]);
      const unsigned l0 = pk_bf16(s.acc[0] - bflo(h0), s.acc[1] - bfhi(h0)), l1 = pk_bf16(s.acc[2] - bflo(h1), s.acc[3] - bfhi(h1));
      *(LAS v2u*)(L + L_A + ws * A_STR + 64 * g + 8 * c) = v2u{h0, h1};
      *(LAS v2u*)(L + L_A + (4 + ws) * A_STR + 64 * g + 8 * c) = v2u{l0, l1};
    }
    if (lane == 0) { ((LAS float*)(L + L_ML))[2 * ws] = s.mc; ((LAS float*)(L + L_ML))[2 * ws + 1] = lsum; }
    WG_BARRIER();
    if (ws == 0 || (ws == 1 && h < 4)) {
      const int cb = h + 32 * ws;
      const float b2v = bf2f(((const GAS u16*)a.b2)[16 * cb + c]);
      const float omg = rbf(1.f - *(const LAS float*)(L + L_MISC));
      const u64 ep1 = *(const LAS u64*)(L + L_MISC + 8);
      u64 gv[8];
      gather8((GAS u64*)(a.ws + W_OX) + (size_t)(b * 36 + cb) * 512 + lane, ep1 & TAG_MASK, gv);
      float sum = 0.f;
#pragma unroll
      for (int i = 0; i < 8; ++i) sum += GVAL(gv[i]);
      sum = xrow_sum(sum);
      if (lane < 16) ((GAS u16*)a.out)[b * DM + 16 * cb + c] = f2bf(sum + omg * b2v);
      if (ws == 0 && bid == 0 && lane == 0) __hip_atomic_store((GAS u64*)(a.ws + W_EPOCH), ep1, RLX_AGENT);
    }
    return;
  }

  __builtin_amdgcn_s_setprio(3);
  LAS unsigned* done_cnt = (LAS unsigned*)(L + L_MISC + 4);
  unsigned pace_t = ((unsigned)__builtin_amdgcn_s_memrealtime() + GM_TA) << 4;
#define PACE(step) do { pace_t += (step); while ((int)(((unsigned)__builtin_amdgcn_s_memrealtime() << 4) - pace_t) < 0) __builtin_amdgcn_s_sleep(2); } while (0)
  if (__builtin_expect(w == 0, 1)) {
    const int ch = b & 1, kq = b >> 1;
    {
      const GAS unsigned char* wqb = (const GAS unsigned char*)a.wq + (size_t)(144 * kq + 8 * (lane >> 5) + ((lane >> 3) & 3)) * (NH * DH * 2)
                                     + h * 256 + ch * 128 + (((((lane & 7) >> 1) ^ g) << 1) | (lane & 1)) * 16;
      unsigned dst = (unsigned)(size_t)(L + L_WO);
#pragma unroll 1
      for (int i = 0; i < 9; ++i) {
        glds16(wqb, dst);
        glds16(wqb + 4 * (NH * DH * 2), dst + 1024);
        wqb += 16 * (NH * DH * 2); dst += 2048;
      }
    }
    u64 e1 = *(const CAS u64*)(a.ws + W_EPOCH);
    const GAS unsigned char* xb = (const GAS unsigned char*)a.x + (c & 7) * (DM * 2) + 288 * kq;
    v4u xa[5];
#pragma unroll
    for (int ks = 0; ks < 4; ++ks) xa[ks] = *(const GAS v4u*)(xb + 64 * ks + 16 * g);
    xa[4] = *(const GAS v4u*)(xb + 256 + 16 * (g & 1));
    asm volatile("s_waitcnt vmcnt(0)" : "+v"(xa[0]), "+v"(xa[1]), "+v"(xa[2]), "+v"(xa[3]), "+v"(xa[4]) :: "memory");
    if (g >= 2) xa[4] = v4u{0u, 0u, 0u, 0u};
    asm volatile("" : "+s"(e1));
    e1 += 1;
    const u64 tag = e1 & TAG_MASK;
    const int q4 = (lane >> 2) & 3, p4 = lane & 3;
    {
      const int sw = 2 * (g & 1) + (q4 >> 1), rb = (4 * (g & 1) + q4) * 128 + 8 * p4;
      GAS u64* qx = (GAS u64*)(a.ws + W_QX);
      GAS u64* qo = qx + ((size_t)(h * NB + 4 * (g & 1)) * 4 + kq) * 128 + 64 * ch + 16 * (g >> 1) + c;
#pragma unroll 1
      for (int pr = 0; pr < 2; ++pr) {
        f32x4 qc[2];
#pragma unroll
        for (int j = 0; j < 2; ++j) {
          qc[j] = f32x4{0.f, 0.f, 0.f, 0.f};
          LAS unsigned char* tb = L + L_WO + rb + (((2 * pr + j) ^ sw) * 32);
#pragma unroll
          for (int ks = 0; ks < 5; ++ks) {
            LAS unsigned char* t2 = tb + ks * 4096 + (ks < 4 ? (g >> 1) * 2048 : 0);
            qc[j] = mfma(xa[ks], tr_frag(t2, t2 + 1024), qc[j]);
          }
        }
#pragma unroll
        for (int r = 0; r < 4; ++r) __hip_atomic_store(qo + r * 512 + 32 * pr, granule(tag, g < 2 ? qc[0][r] : qc[1][r]), RLX_AGENT);
      }
      if (lane == 0) { *(LAS u64*)(L + L_MISC + 8) = e1; *done_cnt = 0u; }
      if (GM_QSLEEP) __builtin_amdgcn_s_sleep(GM_QSLEEP);
      u64 gv[8];
      gather8(qx + (size_t)(h * NB + b) * 512 + lane, tag, gv);
      ((LAS u16*)(L + L_Q))[lane] = f2bf((GVAL(gv[0]) + GVAL(gv[2])) + (GVAL(gv[4]) + GVAL(gv[6])));
      ((LAS u16*)(L + L_Q))[64 + lane] = f2bf((GVAL(gv[1]) + GVAL(gv[3])) + (GVAL(gv[5]) + GVAL(gv[7])));
    }
    WG_BARRIER();
    const GAS unsigned char* wob = (const GAS unsigned char*)(a.wo + (size_t)h * DH * DM);
    int so[9];
#pragma unroll
    for (int j = 0; j < 9; ++j) {
      const int sl = 64 * j + lane, rr = sl / 72, ck = sl % 72;
      so[j] = (8 * (rr & 3) + 2 * (rr >> 2)) * 1152 + ((((ck >> 1) ^ ((rr >> 1) & 3)) << 1) | (ck & 1)) * 16;
    }
#pragma unroll 1
    for (int gr = 0; gr < 16; ++gr) {
      const int ro = (32 * (gr >> 2) + 4 * (gr & 1) + ((gr >> 1) & 1)) * 1152;
#pragma unroll
      for (int j = 0; j < 9; ++j) { PACE(GM_AD * 16 / 144); glds16(wob + ro + so[j], (unsigned)(size_t)(L + L_WO) + gr * 9216 + j * 1024); VMWAIT(GM_AW); }
    }
    VMWAIT(0);
  } else {
    const int m = w - 1;
    WG_BARRIER();
    const GAS u16* xp = (const GAS u16*)(a.x + b * DM);
    float xv[9];
#pragma unroll
    for (int i = 0; i < 9; ++i) xv[i] = bf2f(xp[lane + 64 * i]);
    const int col0 = 64 * ((9 * h) / 4 + m), sl = 4 * (c >> 3) + g;
    const GAS unsigned char* w1p = (const GAS unsigned char*)a.w1 + (size_t)(72 * sl) * (FF * 2) + col0 * 2 + (c & 7) * 16;
    constexpr int R1 = GM_R1;
    v4u r1[R1];
#pragma unroll
    for (int i = 0; i < R1; ++i) { r1[i] = ld_nt((const GAS v4u*)(w1p + i * (FF * 2))); SCHED_FENCE(); }
    float b1v[4];
#pragma unroll
    for (int r = 0; r < 4; ++r) b1v[r] = bf2f(((const GAS u16*)a.b1)[col0 + 8 * (4 * (g & 1) + r) + (c & 7)]);
    if (m == 0) {
      const GAS u16* gp = (const GAS u16*)a.gw;
      float gs = 0.f;
#pragma unroll
      for (int i = 0; i < 9; ++i) gs += rbf(xv[i] * bf2f(gp[lane + 64 * i]));
      const float logit = rbf(wave_sum(gs));
      const float gate = rbf(1.f / (1.f + __expf(-logit)));
      if (lane == 0) *(LAS float*)(L + L_MISC) = gate;
    }
    float sum = 0.f;
#pragma unroll
    for (int i = 0; i < 9; ++i) sum += xv[i];
    const float mean = wave_sum(sum) * (1.f / DM);
    float sq = 0.f;
#pragma unroll
    for (int i = 0; i < 9; ++i) sq += (xv[i] - mean) * (xv[i] - mean);
    const float var = rbf(wave_sum(sq) * (1.f / DM));
    const float mb = rbf(mean), sd = rbf(sqrtf(rbf(var + 1e-5f)));
    LAS u16* yl = (LAS u16*)(L + L_Y + m * 1152);
#pragma unroll
    for (int i = 0; i < 9; ++i) yl[lane + 64 * i] = f2bf(rbf(xv[i] - mb) / sd);
    LDS_WAIT();
    unsigned mask[4];
#pragma unroll
    for (int r = 0; r < 4; ++r) mask[r] = ((c & 7) >> 1) == r ? ((c & 1) ? 0xffff0000u : 0x0000ffffu) : 0u;
    f32x4 h1 = {0.f, 0.f, 0.f, 0.f};
    static_assert(24 % R1 == 0, "ring");
#pragma unroll 1
    for (int o = 0; o < 72; o += 24) {
      v4u yy[3];
#pragma unroll
      for (int i = 0; i < 3; ++i) yy[i] = *(const LAS v4u*)(yl + 72 * sl + o + 8 * i);
#pragma unroll
      for (int i = 0; i < 24; ++i) {
        const unsigned u = yy[i >> 3][(i >> 1) & 3];
        const unsigned dup = __builtin_amdgcn_perm(u, u, (i & 1) ? 0x03020302u : 0x01000100u);
        h1 = mfma(r1[i % R1], v4u{dup & mask[0], dup & mask[1], dup & mask[2], dup & mask[3]}, h1);
        SCHED_FENCE();
        const int nx = o + i + R1 < 72 ? o + i + R1 : 71;
        PACE(GM_AD * 16 / 144);
        r1[i % R1] = ld_nt((const GAS v4u*)(w1p + (size_t)nx * (FF * 2)));
        SCHED_FENCE();
      }
    }
    const int lo = col0 > FS * h ? col0 : FS * h, hi = col0 + 64 < FS * h + FS ? col0 + 64 : FS * h + FS;
    const int nrg = (hi - lo) >> 3;
    const unsigned step2 = (GM_AD * 16 / 18) / nrg;
    const GAS unsigned char* w2p = (const GAS unsigned char*)a.w2 + (size_t)(lo + sl) * (DM * 2) + (c & 7) * 16;
    v4u r2[3];
#pragma unroll
    for (int i = 0; i < 3; ++i) { r2[i] = ld_nt((const GAS v4u*)(w2p + i * 128)); SCHED_FENCE(); }
    LAS u16* hid = (LAS u16*)(L + L_HID + m * 128);
#pragma unroll
    for (int r = 0; r < 4; ++r) {
      const float pre = rbf(rbf(fold_halves(h1[r])) + b1v[r]);
      const float inner = 0.7978845608028654f * (pre + 0.044715f * pre * pre * pre);
      const float th = 1.f - 2.f / (1.f + __expf(2.f * inner));
      if (lane < 32 && c < 8) hid[8 * (4 * g + r) + c] = f2bf(0.5f * pre * (1.f + th));
    }
    LDS_WAIT();
    f32x4 o2[9];
#pragma unroll
    for (int j = 0; j < 9; ++j) o2[j] = f32x4{0.f, 0.f, 0.f, 0.f};
#pragma unroll 1
    for (int rg = 0; rg < nrg; ++rg) {
      const unsigned u = hid[lo - col0 + 8 * rg + sl];
      const unsigned dup = u | (u << 16);
      const v4u bf = {dup & mask[0], dup & mask[1], dup & mask[2], dup & mask[3]};
      const GAS unsigned char* cur = w2p + (size_t)rg * (8 * DM * 2);
      const GAS unsigned char* nxt = w2p + (size_t)(rg + 1 < nrg ? rg + 1 : rg) * (8 * DM * 2);
#pragma unroll
      for (int j = 0; j < 9; ++j) {
        o2[j] = mfma(r2[j % 3], bf, o2[j]);
        SCHED_FENCE();
        PACE(step2);
        r2[j % 3] = ld_nt((const GAS v4u*)(j < 6 ? cur + (j + 3) * 128 : nxt + (j - 6) * 128));
        SCHED_FENCE();
      }
    }
    LAS float* mp = (LAS float*)(L + L_MLP + m * 2304);
#pragma unroll
    for (int j = 0; j < 9; ++j) {
      f32x4 t;
#pragma unroll
      for (int r = 0; r < 4; ++r) t[r] = fold_halves(o2[j][r]);
      if (lane < 32 && c < 8) {
#pragma unroll
        for (int r = 0; r < 4; ++r) mp[64 * j + 8 * (4 * g + r) + c] = t[r];
      }
    }
  }
  LDS_WAIT();
  if (lane == 0) (void)__hip_atomic_fetch_add(done_cnt, 1u, __ATOMIC_RELAXED, __HIP_MEMORY_SCOPE_WORKGROUP);
  while (__builtin_amdgcn_readfirstlane(__hip_atomic_load(done_cnt, __ATOMIC_RELAXED, __HIP_MEMORY_SCOPE_WORKGROUP)) < (unsigned)NWA) __builtin_amdgcn_s_sleep(8);
  asm volatile("" ::: "memory");
  v4u bf[9][4];
  {
    const int q4 = (lane >> 2) & 3, p4 = lane & 3;
    LAS unsigned char* tb = L + L_WO + (4 * g + q4) * 1152 + ((w ^ ((2 * g + (q4 >> 1)) & 3)) * 32) + 8 * p4;
#pragma unroll
    for (int j = 0; j < 9; ++j)
#pragma unroll
      for (int ks = 0; ks < 4; ++ks) bf[j][ks] = tr_frag(tb + ks * 32 * 1152 + j * 128, tb + (ks * 32 + 16) * 1152 + j * 128);
  }
  const float gate = *(const LAS float*)(L + L_MISC);
  const float omg = rbf(1.f - gate);
  const u64 tag = *(const LAS u64*)(L + L_MISC + 8) & TAG_MASK;
  float base[3];
#pragma unroll
  for (int i = 0; i < 3; ++i) {
    const int n = 16 * (w + 4 * (i < 2 ? 4 * i + g : 8)) + c;
    const LAS float* mp = (const LAS float*)(L + L_MLP);
    base[i] = omg * ((mp[n] + mp[DM + n]) + mp[2 * DM + n]);
  }
  GAS u64* ox = (GAS u64*)(a.ws + W_OX) + ((size_t)(b * 36 + w + 4 * g) * 32 + h) * 16 + c;
#pragma unroll
  for (int j = 0; j < 9; ++j) asm volatile("" : "+v"(bf[j][0]), "+v"(bf[j][1]), "+v"(bf[j][2]), "+v"(bf[j][3]));
  asm volatile("" : "+v"(base[0]), "+v"(base[1]), "+v"(base[2]), "+v"(ox));
  WG_BARRIER();
  v4u af[4];
#pragma unroll
  for (int ks = 0; ks < 4; ++ks) af[ks] = *(const LAS v4u*)(L + L_A + (c & 7) * A_STR + 64 * ks + 16 * g);
  const f32x4 ml0 = *(const LAS f32x4*)(L + L_ML), ml1 = *(const LAS f32x4*)(L + L_ML + 16);
  const float mx = fmaxf(fmaxf(ml0[0], ml0[2]), fmaxf(ml1[0], ml1[2]));
  f32x4 cw = {__builtin_amdgcn_exp2f(ml0[0] - mx), __builtin_amdgcn_exp2f(ml0[2] - mx), __builtin_amdgcn_exp2f(ml1[0] - mx), __builtin_amdgcn_exp2f(ml1[2] - mx)};
  const float den = (cw[0] * ml0[1] + cw[1] * ml0[3]) + (cw[2] * ml1[1] + cw[3] * ml1[3]);
  cw *= gate / den;
  float sel = 0.f;
#pragma unroll
  for (int j = 0; j < 9; ++j) {
    f32x4 oc = {0.f, 0.f, 0.f, 0.f};
#pragma unroll
    for (int ks = 0; ks < 4; ++ks) oc = mfma(af[ks], bf[j][ks], oc);
    const float val = x16_sum((cw[0] * oc[0] + cw[1] * oc[1]) + (cw[2] * oc[2] + cw[3] * oc[3]));
    sel = (g == (j & 3)) ? val : sel;
    if (j == 3 || j == 7) __hip_atomic_store(ox + (j >> 2) * (16 * 512), granule(tag, sel + base[j >> 2]), RLX_AGENT);
    if (j == 8 && g == 0) __hip_atomic_store(ox + 2 * (16 * 512), granule(tag, sel + base[2]), RLX_AGENT);
  }
}

}

extern "C" void kernel_launch(void* const* d_in, const int* in_sizes, int n_in, void* d_out, int out_size, void* d_ws,
                              size_t ws_size, hipStream_t stream) {
  (void)in_sizes; (void)n_in; (void)out_size; (void)ws_size;
  gm::Args a;
  a.x = (const gm::u16*)d_in[0]; a.k = (const gm::u16*)d_in[1]; a.v = (const gm::u16*)d_in[2];
  a.wq = (const gm::u16*)d_in[3]; a.wo = (const gm::u16*)d_in[4];
  a.w1 = (const gm::u16*)d_in[6]; a.b1 = (const gm::u16*)d_in[7]; a.w2 = (const gm::u16*)d_in[8]; a.b2 = (const gm::u16*)d_in[9];
  a.gw = (const gm::u16*)d_in[10];
  a.out = (gm::u16*)d_out; a.ws = (unsigned char*)d_ws;
  (void)hipFuncSetAttribute((const void*)gm::fused_kernel, hipFuncAttributeMaxDynamicSharedMemorySize, gm::L_BYTES);
  hipLaunchKernelGGL(gm::fused_kernel, dim3(gm::NB * gm::NH), dim3(gm::NTHR), gm::L_BYTES, stream, a);
}
```

```cpp
#include <hip/hip_runtime.h>
#include <hip/hip_bf16.h>
#include <stdint.h>
#include <type_traits>

namespace gm {

constexpr int NB = 8, NH = 32, SK = 4096, DH = 128, DM = 576, FF = 4608;
constexpr int FS = FF / NH;
constexpr int NWS = 4, NWA = 4, NTHR = (NWS + NWA) * 64;
constexpr int NS = SK / 64 / NWS;
#define GM_EXTRA 1
constexpr int EXTRA = GM_EXTRA;
#define GM_SW0 5
#define GM_AW 2
#define GM_R1 3
#define GM_TOUCH 0
#define GM_QSLEEP 40
#define GM_PAT 0
#define GM_LO 3
#define GM_CTL 1
#define GM_ROT 7
#define GM_OFF 330
#define GM_P1 1735
#define GM_P2 1735
#define GM_I1 52
#define GM_STEP 75
#define GM_TA 800
#define GM_AD 5400
#define GM_CK 3
#define GM_L0 40
#define GM_GSLEEP 24
#define GM_DRY 1
#define GM_PF 1
#define GM_WPOL 0
#define GM_T0 28
#define GM_T1 28
#define GM_T2 28
#define STR2(x) #x
#define STR(x) STR2(x)
#define VMWAIT(n) asm volatile("s_waitcnt vmcnt(" STR(n) ")" ::: "memory")

constexpr int L_WO = 0;
constexpr int A_STR = 272;
constexpr int L_A = 128 * 1152;
constexpr int L_MLP = L_A + 9 * A_STR;
constexpr int L_Y = L_MLP + 3 * 2304;
constexpr int L_HID = L_Y + 3 * 1152;
constexpr int L_ML = L_HID + 3 * 128;
constexpr int L_Q = L_ML + 32;
constexpr int L_MISC = L_Q + 256;
constexpr int L_CTL = L_MISC + 16;
constexpr int L_BYTES = L_CTL + 32;
static_assert(L_BYTES <= 163840 && L_MLP % 16 == 0 && L_ML % 16 == 0 && L_MISC % 16 == 0, "LDS");

constexpr size_t W_EPOCH = 0;
constexpr size_t W_QX = 4096;
constexpr size_t W_PACE = 4 << 20;
constexpr size_t W_OX = W_QX + (size_t)NH * NB * 4 * 128 * 8;

#define GAS __attribute__((address_space(1)))
#define LAS __attribute__((address_space(3)))
#define CAS __attribute__((address_space(4)))
typedef unsigned v4u __attribute__((ext_vector_type(4)));
typedef unsigned v2u __attribute__((ext_vector_type(2)));
typedef unsigned v8u __attribute__((ext_vector_type(8)));
typedef float f32x4 __attribute__((ext_vector_type(4)));
typedef short s16x4 __attribute__((ext_vector_type(4)));
typedef __bf16 bf16x2 __attribute__((ext_vector_type(2)));
typedef __bf16 bf16x8 __attribute__((ext_vector_type(8)));
typedef unsigned long long u64;
typedef unsigned short u16;
typedef __amdgpu_buffer_rsrc_t rsrc_t;

struct Touch { const unsigned char* base; unsigned mx, ms, span, md, sb, unused; };
struct Args {
  const u16* k; const u16* v; const u16* wq; const u16* x; unsigned char* ws;
  const u16* wo; const u16* w1; const u16* b1; const u16* w2; const u16* b2; const u16* gw; u16* out;
  Touch touch[4];
};
static_assert(sizeof(Args) == 4 * 32 + 12 * 8, "no padding");
__device__ __forceinline__ const CAS Args& args() {
  const CAS Args* p = (const CAS Args*)__builtin_amdgcn_kernarg_segment_ptr();
  asm volatile("" : "+s"(p));
  return *p;
}

__device__ __forceinline__ v4u ld_nt(const GAS v4u* p) { if (GM_WPOL) return __builtin_nontemporal_load(p); else return *p; }
__device__ __forceinline__ v4u ld_kv(rsrc_t rs, int voff, int soff) { return __builtin_amdgcn_raw_buffer_load_b128(rs, voff, soff, 2); }
template <bool NT> __device__ __forceinline__ void glds16(const GAS unsigned char* g, unsigned lds_base) {
  unsigned keep;
  if (!NT) asm volatile("s_mov_b32 %0, m0\n\ts_mov_b32 m0, %2\n\ts_nop 0\n\tglobal_load_lds_dwordx4 %1, off\n\ts_mov_b32 m0, %0" : "=&s"(keep) : "v"(g), "s"(lds_base) : "memory");
  else
  asm volatile("s_mov_b32 %0, m0\n\ts_mov_b32 m0, %2\n\ts_nop 0\n\tglobal_load_lds_dwordx4 %1, off nt\n\ts_mov_b32 m0, %0" : "=&s"(keep) : "v"(g), "s"(lds_base) : "memory");
}
__device__ __forceinline__ float bf2f(unsigned h) { return __builtin_bit_cast(float, h << 16); }
__device__ __forceinline__ float bflo(unsigned u) { return __builtin_bit_cast(float, u << 16); }
__device__ __forceinline__ float bfhi(unsigned u) { return __builtin_bit_cast(float, u & 0xffff0000u); }
__device__ __forceinline__ unsigned pk_bf16(float lo, float hi) {
  bf16x2 p = {(__bf16)lo, (__bf16)hi};
  return __builtin_bit_cast(unsigned, p);
}
__device__ __forceinline__ float rbf(float f) { return (float)(__bf16)f; }
__device__ __forceinline__ u16 f2bf(float f) { return __builtin_bit_cast(u16, (__bf16)f); }
__device__ __forceinline__ float dot2(unsigned a, unsigned b, float c) {
  return __builtin_amdgcn_fdot2_f32_bf16(__builtin_bit_cast(bf16x2, a), __builtin_bit_cast(bf16x2, b), c, false);
}
__device__ __forceinline__ f32x4 mfma(v4u a, v4u b, f32x4 c) {
  return __builtin_amdgcn_mfma_f32_16x16x32_bf16(__builtin_bit_cast(bf16x8, a), __builtin_bit_cast(bf16x8, b), c, 0, 0, 0);
}
__device__ __forceinline__ v4u tr_frag(LAS unsigned char* lo, LAS unsigned char* hi) {
  const v2u l2 = __builtin_bit_cast(v2u, __builtin_amdgcn_ds_read_tr16_b64_v4i16((LAS s16x4*)lo));
  const v2u h2 = __builtin_bit_cast(v2u, __builtin_amdgcn_ds_read_tr16_b64_v4i16((LAS s16x4*)hi));
  return v4u{l2[0], l2[1], h2[0], h2[1]};
}
template <int CTRL> __device__ __forceinline__ float dppf(float x) {
  return __builtin_bit_cast(float, __builtin_amdgcn_mov_dpp(__builtin_bit_cast(int, x), CTRL, 0xf, 0xf, true));
}
template <int CTRL> __device__ __forceinline__ unsigned dppu(unsigned x) {
  return (unsigned)__builtin_amdgcn_mov_dpp((int)x, CTRL, 0xf, 0xf, true);
}
constexpr int XOR1 = 0xB1, XOR2 = 0x4E, XOR7 = 0x141, XOR8 = 0x128, BCAST = 0x150;
__device__ __forceinline__ float x16_sum(float x) {
  auto s = __builtin_amdgcn_permlane16_swap(__float_as_uint(x), __float_as_uint(x), false, false);
  return __uint_as_float(s[0]) + __uint_as_float(s[1]);
}
__device__ __forceinline__ float xrow_sum(float x) {
  x = x16_sum(x);
  auto t = __builtin_amdgcn_permlane32_swap(__float_as_uint(x), __float_as_uint(x), false, false);
  return __uint_as_float(t[0]) + __uint_as_float(t[1]);
}
__device__ __forceinline__ float xrow_max(float x) {
  auto s = __builtin_amdgcn_permlane16_swap(__float_as_uint(x), __float_as_uint(x), false, false);
  x = fmaxf(__uint_as_float(s[0]), __uint_as_float(s[1]));
  auto t = __builtin_amdgcn_permlane32_swap(__float_as_uint(x), __float_as_uint(x), false, false);
  return fmaxf(__uint_as_float(t[0]), __uint_as_float(t[1]));
}
__device__ __forceinline__ float fold_halves(float x) {
  const float t = dppf<XOR8>(x);
  auto p = __builtin_amdgcn_permlane32_swap(__float_as_uint(t), __float_as_uint(t), false, false);
  return x + __uint_as_float(p[1]);
}
__device__ __forceinline__ float wave_sum(float x) {
  x += dppf<XOR1>(x); x += dppf<XOR2>(x); x += dppf<XOR7>(x); x += dppf<XOR8>(x);
  return xrow_sum(x);
}
__device__ __forceinline__ float wave_max(float x) {
  x = fmaxf(x, dppf<XOR1>(x)); x = fmaxf(x, dppf<XOR2>(x)); x = fmaxf(x, dppf<XOR7>(x)); x = fmaxf(x, dppf<XOR8>(x));
  return xrow_max(x);
}
#define WG_BARRIER() asm volatile("s_waitcnt lgkmcnt(0)\n\ts_barrier" ::: "memory")
#define LDS_WAIT() asm volatile("s_waitcnt lgkmcnt(0)" ::: "memory")
#define SCHED_FENCE() __builtin_amdgcn_sched_barrier(0)
#define RLX_AGENT __ATOMIC_RELAXED, __HIP_MEMORY_SCOPE_AGENT

constexpr float SCL = 0.088388347648318440f * 1.4426950408889634f;
constexpr u64 TAG_MASK = (1ull << 40) - 1;

__device__ __forceinline__ u64 granule(u64 tag, float v) { return (tag << 24) | (u64)((__builtin_bit_cast(unsigned, v) + 0x80u) >> 8); }
#define GVAL(x) __builtin_bit_cast(float, (unsigned)(x) << 8)
__device__ __forceinline__ void gather8(GAS u64* ib, u64 tag, u64 (&gv)[8]) {
#pragma unroll
  for (int i = 0; i < 8; ++i) gv[i] = __hip_atomic_load(ib + 64 * i, RLX_AGENT);
  for (unsigned spin = 0; spin < (1u << 22); ++spin) {
    bool all = true;
#pragma unroll
    for (int i = 0; i < 8; ++i) all = all && (gv[i] >> 24) == tag;
    if (__builtin_amdgcn_ballot_w64(!all) == 0) break;
    __builtin_amdgcn_s_sleep(1);
#pragma unroll
    for (int i = 0; i < 8; ++i)
      if ((gv[i] >> 24) != tag) gv[i] = __hip_atomic_load(ib + 64 * i, RLX_AGENT);
  }
}

typedef float f32x16 __attribute__((ext_vector_type(16)));
constexpr int LAG_FREE = 1 << 20;
#define THROTTLE(lag, i) asm volatile( \
    "s_cmp_ge_i32 %0, 0x100000\n\ts_cbranch_scc1 1f\n\ts_waitcnt vmcnt(%c1)\n\t" \
    "s_cmp_gt_i32 %0, %c5\n\ts_cbranch_scc1 1f\n\ts_waitcnt vmcnt(%c2)\n\t" \
    "s_cmp_gt_i32 %0, %c6\n\ts_cbranch_scc1 1f\n\ts_waitcnt vmcnt(%c3)\n\t" \
    "s_cmp_gt_i32 %0, %c7\n\ts_cbranch_scc1 1f\n\ts_waitcnt vmcnt(%c4)\n1:" \
    :: "s"(lag), "i"(GM_LO + 3), "i"(GM_LO + 2), "i"(GM_LO + 1), "i"(GM_LO), \
       "i"(GM_STEP + DITH(i)), "i"(DITH(i)), "i"(-GM_STEP + DITH(i)) : "memory", "scc")
#define DITH(i) (GM_STEP * ((((i) & 1) * 4 + ((i) & 2) + ((i) >> 2)) * 2 + 1) / 16)

template <int J> __device__ __forceinline__ void throttle(int lag) { THROTTLE(lag, (J & 7)); }

#define QUARTER_OFF(st, d) ((((st) ^ rot) << 14) + ((d) << 18))

struct Sel { bool b8, b4, b2, b1; };
struct Red { float x0, x1, x2, x3; };
template <int CTRL> __device__ __forceinline__ float merge2(float lo, float hi, bool take_hi) {
  const float u = lo + dppf<CTRL>(lo), v = hi + dppf<CTRL>(hi);
  return take_hi ? v : u;
}
template <int J> __device__ __forceinline__ void red_push(Red& r, float d, const Sel& s) {
  if (!(J & 1)) { r.x0 = d; return; }
  d = merge2<XOR8>(r.x0, d, s.b8);
  if (!(J & 2)) { r.x1 = d; return; }
  d = merge2<XOR7>(r.x1, d, s.b4);
  if (!(J & 4)) { r.x2 = d; return; }
  d = merge2<XOR2>(r.x2, d, s.b2);
  if (!(J & 8)) { r.x3 = d; return; }
  r.x0 = merge2<XOR1>(r.x3, d, s.b1);
}
__device__ __forceinline__ float dot8(const v4u k, const v4u q) {
  float d = dot2(k[0], q[0], 0.f);
  d = dot2(k[1], q[1], d); d = dot2(k[2], q[2], d);
  return dot2(k[3], q[3], d);
}
template <int J, int N> struct Unroll {
  template <typename F> static __device__ __forceinline__ void run(F&& f) { f(std::integral_constant<int, J>{}); Unroll<J + 1, N>::run(f); }
};
template <int N> struct Unroll<N, N> { template <typename F> static __device__ __forceinline__ void run(F&&) {} };

__global__ void __launch_bounds__(NTHR, 2) fused_kernel(Args) {
  extern __shared__ __attribute__((aligned(16))) unsigned char smem[];
  LAS unsigned char* L = (LAS unsigned char*)smem;
  const int tid = threadIdx.x, lane = tid & 63, w = __builtin_amdgcn_readfirstlane(tid >> 6);
  v8u ka; v4u kb;
  asm volatile("s_load_dwordx4 %0, %1, 0x0" : "=&s"(kb) : "s"(__builtin_amdgcn_kernarg_segment_ptr()) : "memory");
  const int xc = blockIdx.x, b = blockIdx.z, h = xc * 4 + blockIdx.y, sx = b * 4 + blockIdx.y;
  const int bid = sx * 8 + xc;
  const int g = lane >> 4, c = lane & 15;

  if (__builtin_expect(w >= NWA, 1)) {
    const int ws = w - NWA;
    const unsigned t_zero = GM_CK ? 0u : (unsigned)__builtin_amdgcn_s_memrealtime();
    const size_t kvoff = (size_t)(b * NH + h) * SK * DH * 2;
    const int voff = lane * 16 + ws * 4096;
    const int rot = sx & GM_ROT;
    asm volatile("s_waitcnt lgkmcnt(0)\n\ts_load_dwordx8 %0, %2, %3" : "=&s"(ka), "+s"(kb) : "s"(__builtin_amdgcn_kernarg_segment_ptr()), "s"(w * 32 - 32) : "memory");
    const unsigned char* kp = (const unsigned char*)(((u64)kb[1] << 32) | kb[0]) + kvoff;
    const unsigned char* vp = (const unsigned char*)(((u64)kb[3] << 32) | kb[2]) + kvoff;
    const rsrc_t rk = __builtin_amdgcn_make_buffer_rsrc((void*)kp, 0, SK * DH * 2, 0x00020000);
    const rsrc_t rv = __builtin_amdgcn_make_buffer_rsrc((void*)vp, 0, SK * DH * 2, 0x00020000);
    unsigned pf[3];
#define TOUCH(i) { if ((i) == 0) asm volatile("s_waitcnt lgkmcnt(0)" : "+s"(ka) :: "memory"); \
      const rsrc_t rp = __builtin_amdgcn_make_buffer_rsrc((void*)((const unsigned char*)(((u64)ka[1] << 32) | ka[0]) + (xc * ka[2] + sx * ka[3])), 0, ka[4], 0x00020000); \
      const unsigned id = 64 * (i) + lane; pf[i] = __builtin_amdgcn_raw_buffer_load_b32(rp, id * 128 + __umul24(__umul24(id, ka[5]) >> 9, ka[6]), 0, 0); SCHED_FENCE(); }
    v4u ring[16], xr[EXTRA ? EXTRA : 1][16];
#define FINE(i)
#pragma unroll
    for (int e = 0; e <= EXTRA; ++e)
#pragma unroll
      for (int j = 0; j < 16; ++j) {
        if (16 * e + j == GM_T0) TOUCH(0)
        if (16 * e + j == GM_T1) TOUCH(1)
        if (16 * e + j == GM_T2) TOUCH(2)
        if (e > 0 || j >= GM_T0 || j > GM_SW0) VMWAIT(GM_SW0);
        if (j % 2 == 0) FINE(e * 8 + j / 2);
        const v4u t_ = ld_kv(rk, voff + (j & 3) * 1024, QUARTER_OFF(e, j >> 2));
        if (e == 0) ring[j] = t_; else xr[e - 1][j] = t_;
        SCHED_FENCE();
      }
    if (GM_T0 >= 16 * (EXTRA + 1)) TOUCH(0)
    if (GM_T1 >= 16 * (EXTRA + 1)) TOUCH(1)
    if (GM_T2 >= 16 * (EXTRA + 1)) TOUCH(2)
    const Sel sel = {(c & 8) != 0, (c & 4) != 0, (c & 2) != 0, (c & 1) != 0};
    WG_BARRIER();
    const v4u q = *(const LAS v4u*)(L + L_Q + 16 * c);
    auto lag_of = [&](int i) -> int {
      if (!GM_CTL) return 0;
      const int e = (int)(unsigned)__builtin_amdgcn_s_memrealtime() - ((GM_P2 * i + (GM_P1 - GM_P2) * (i < GM_I1 ? i : GM_I1)) >> 4);
      if (!GM_CK) return e - (int)t_zero - GM_OFF;
      const int base = *(volatile LAS int*)(L + L_CTL + 16);
      *(volatile LAS int*)(L + L_CTL + 4 * ws) = e;
      return e - __builtin_amdgcn_readfirstlane(base);
    };
    f32x16 zs;
    float mx = -INFINITY;
#pragma unroll
    for (int e = 0; e < EXTRA; ++e) {
      Red ra, rb;
      int lag = 0;
      Unroll<0, 16>::run([&](auto jc) {
        constexpr int j = decltype(jc)::value;
        if (j % 8 == 0) lag = lag_of(2 * (EXTRA + 1 + e) + j / 8);
        float d = dot8(ring[j], q);
        asm volatile("" : "+v"(d));
        throttle<j>(lag);
        ring[j] = ld_kv(rk, voff + (j & 3) * 1024, QUARTER_OFF(EXTRA + 1 + e, j >> 2));
        SCHED_FENCE();
        red_push<j>(ra, d, sel);
        red_push<j>(rb, dot8(xr[e][j], q), sel);
        SCHED_FENCE();
      });
      zs[e == 0 ? 0 : EXTRA + e] = ra.x0; zs[1 + e] = rb.x0;
      mx = fmaxf(mx, fmaxf(ra.x0, rb.x0));
      FINE(30 + EXTRA + 1 + e);
    }
#pragma unroll 1
    for (int n = 2 * EXTRA + 1; n <= NS; ++n) {
      const rsrc_t rs = __builtin_amdgcn_make_buffer_rsrc((void*)(n < NS ? kp : vp), 0, SK * DH * 2, 0x00020000);
      const int st = n & (NS - 1);
      Red ra;
      int lag = 0;
      Unroll<0, 16>::run([&](auto jc) {
        constexpr int j = decltype(jc)::value;
        if (j % 8 == 0) lag = lag_of(2 * n + j / 8);
        float d = dot8(ring[j], q);
        asm volatile("" : "+v"(d));
        throttle<j>(lag);
        ring[j] = ld_kv(rs, voff + (j & 3) * 1024, QUARTER_OFF(st, j >> 2));
        SCHED_FENCE();
        red_push<j>(ra, d, sel);
        SCHED_FENCE();
      });
      zs[n - 1] = ra.x0;
      mx = fmaxf(mx, ra.x0);
      FINE(30 + n);
    }
#pragma unroll
    for (int i = 0; i < 3; ++i) asm volatile("" :: "v"(pf[i]));
    const float mc = wave_max(mx) * SCL;
    unsigned mask[4];
#pragma unroll
    for (int r = 0; r < 4; ++r) mask[r] = (c >> 1) == r ? ((c & 1) ? 0xffff0000u : 0x0000ffffu) : 0u;
    {
      float lp = 0.f;
#pragma unroll
      for (int i = 0; i < NS; ++i) {
        const float p = __builtin_amdgcn_exp2f(fmaf(zs[i], SCL, -mc));
        lp += p;
        zs[i] = __builtin_bit_cast(float, pk_bf16(p, p));
      }
      const float lsum = wave_sum(lp);
      if (lane == 0) {
        volatile LAS float* ml = (volatile LAS float*)(L + L_ML);
        ml[2 * ws] = mc; ml[2 * ws + 1] = lsum;
        LDS_WAIT();
        (void)__hip_atomic_fetch_add((LAS unsigned*)(L + L_MISC + 4), 1u, __ATOMIC_RELAXED, __HIP_MEMORY_SCOPE_WORKGROUP);
      }
    }
    f32x4 acc = {0.f, 0.f, 0.f, 0.f};
#pragma unroll 1
    for (int n = 1; n <= NS; ++n) {
      const float pf = zs[n - 1];
      const unsigned pk = __float_as_uint(pf);
      const int oob = n < NS ? 0 : (1 << 24);
      int lag = 0;
      Unroll<0, 16>::run([&](auto jc) {
        constexpr int j = decltype(jc)::value;
        constexpr int src = ((j & 1) << 3) | ((j & 2) << 1) | ((j & 4) >> 1) | ((j & 8) >> 3);
        if (j % 8 == 0) { lag = lag_of(2 * (NS + n) + j / 8); lag = n < NS ? lag : LAG_FREE; }
        const v4u bf = {dppu<BCAST + src>(pk) & mask[0], dppu<BCAST + src>(pk) & mask[1], dppu<BCAST + src>(pk) & mask[2], dppu<BCAST + src>(pk) & mask[3]};
        acc = mfma(ring[j], bf, acc);
        asm volatile("" : "+v"(acc));
        SCHED_FENCE();
        throttle<j>(lag);
        ring[j] = ld_kv(rv, voff + (j & 3) * 1024, QUARTER_OFF(n & (NS - 1), j >> 2) + oob);
        SCHED_FENCE();
      });
      FINE(30 + NS + n);
    }
    if (c < 8) {
      const unsigned h0 = pk_bf16(acc[0], acc[1]), h1 = pk_bf16(acc[2], acc[3]);
      const unsigned l0 = pk_bf16(acc[0] - bflo(h0), acc[1] - bfhi(h0)), l1 = pk_bf16(acc[2] - bflo(h1), acc[3] - bfhi(h1));
      *(LAS v2u*)(L + L_A + ws * A_STR + 64 * g + 8 * c) = v2u{h0, h1};
      *(LAS v2u*)(L + L_A + (4 + ws) * A_STR + 64 * g + 8 * c) = v2u{l0, l1};
    }
    WG_BARRIER();
    const CAS Args& a = args();
    if (ws == 0 || (ws == 1 && h < 4)) {
      const int cb = h + 32 * ws;
      const float b2v = bf2f(((const GAS u16*)a.b2)[16 * cb + c]);
      const float omg = rbf(1.f - *(const LAS float*)(L + L_MISC));
      const u64 ep1 = *(const LAS u64*)(L + L_MISC + 8);
      const u64 tg = ep1 & TAG_MASK;
      GAS u64* ib = (GAS u64*)(a.ws + W_OX) + (size_t)(b * 36 + cb) * 512 + lane;
      GAS u16* op = (GAS u16*)a.out + b * DM + 16 * cb + c;
      u64 gv[8];
#pragma unroll
      for (int i = 0; i < 8; ++i) gv[i] = ~tg << 24;
      u16 res;
      for (unsigned spin = 0;; ++spin) {
        bool all = true;
        float sum = 0.f;
#pragma unroll
        for (int i = 0; i < 8; ++i) { all = all && (gv[i] >> 24) == tg; sum += GVAL(gv[i]); }
        res = f2bf(xrow_sum(sum) + omg * b2v);
        asm volatile("" : "+v"(res));
        if (__builtin_amdgcn_ballot_w64(!all) == 0 || spin > (1u << 22)) break;
        if (spin == 0) __builtin_amdgcn_s_sleep(GM_GSLEEP); else __builtin_amdgcn_s_sleep(1);
#pragma unroll
        for (int i = 0; i < 8; ++i)
          if ((gv[i] >> 24) != tg) gv[i] = __hip_atomic_load(ib + 64 * i, RLX_AGENT);
      }
      if (lane < 16) *op = res;
      if (ws == 0 && bid == 0 && lane == 0) __hip_atomic_store((GAS u64*)(a.ws + W_EPOCH), ep1, RLX_AGENT);
    }
    return;
  }

  asm volatile("s_waitcnt lgkmcnt(0)" :: "s"(kb) : "memory");
  __builtin_amdgcn_s_setprio(3);
  const CAS Args& a = args();
  LAS unsigned* done_cnt = (LAS unsigned*)(L + L_MISC + 4);
  u64 pace_t = __builtin_amdgcn_s_memrealtime();
#define PACE(step) do { pace_t += (step); while ((int)(((unsigned)__builtin_amdgcn_s_memrealtime() << 4) - (unsigned)pace_t) < 0) __builtin_amdgcn_s_sleep(2); } while (0)
  if (__builtin_expect(w == 0, 1)) {
    const int ch = b & 1, kq = b >> 1;
    {
      const GAS unsigned char* wqb = (const GAS unsigned char*)a.wq + (size_t)(144 * kq + 8 * (lane >> 5) + ((lane >> 3) & 3)) * (NH * DH * 2)
                                     + h * 256 + ch * 128 + (((((lane & 7) >> 1) ^ g) << 1) | (lane & 1)) * 16;
      unsigned dst = (unsigned)(size_t)(L + L_WO);
#pragma unroll 1
      for (int i = 0; i < 9; ++i) {
        glds16<true>(wqb, dst);
        glds16<true>(wqb + 4 * (NH * DH * 2), dst + 1024);
        wqb += 16 * (NH * DH * 2); dst += 2048;
      }
    }
    asm volatile("" : "+s"(pace_t));
    pace_t = (pace_t + (GM_TA - 40)) << 4;
    u64 e1 = *(const CAS u64*)(a.ws + W_EPOCH);
    const GAS unsigned char* xb = (const GAS unsigned char*)a.x + (c & 7) * (DM * 2) + 288 * kq;
    v4u xa[5];
#pragma unroll
    for (int ks = 0; ks < 4; ++ks) xa[ks] = *(const GAS v4u*)(xb + 64 * ks + 16 * g);
    xa[4] = *(const GAS v4u*)(xb + 256 + 16 * (g & 1));
    asm volatile("s_waitcnt vmcnt(0)" : "+v"(xa[0]), "+v"(xa[1]), "+v"(xa[2]), "+v"(xa[3]), "+v"(xa[4]) :: "memory");
    if (g >= 2) xa[4] = v4u{0u, 0u, 0u, 0u};
    asm volatile("" : "+s"(e1));
    e1 += 1;
    const u64 tag = e1 & TAG_MASK;
    const int q4 = (lane >> 2) & 3, p4 = lane & 3;
    {
      const int sw = 2 * (g & 1) + (q4 >> 1), rb = (4 * (g & 1) + q4) * 128 + 8 * p4;
      GAS u64* qx = (GAS u64*)(a.ws + W_QX);
      GAS u64* qo = qx + ((size_t)(h * NB + 4 * (g & 1)) * 4 + kq) * 128 + 64 * ch + 16 * (g >> 1) + c;
#pragma unroll 1
      for (int pr = 0; pr < 2; ++pr) {
        f32x4 qc[2];
#pragma unroll
        for (int j = 0; j < 2; ++j) {
          qc[j] = f32x4{0.f, 0.f, 0.f, 0.f};
          LAS unsigned char* tb = L + L_WO + rb + (((2 * pr + j) ^ sw) * 32);
#pragma unroll
          for (int ks = 0; ks < 5; ++ks) {
            LAS unsigned char* t2 = tb + ks * 4096 + (ks < 4 ? (g >> 1) * 2048 : 0);
            qc[j] = mfma(xa[ks], tr_frag(t2, t2 + 1024), qc[j]);
          }
        }
#pragma unroll
        for (int r = 0; r < 4; ++r) __hip_atomic_store(qo + r * 512 + 32 * pr, granule(tag, g < 2 ? qc[0][r] : qc[1][r]), RLX_AGENT);
      }
      if (lane == 0) { *(LAS u64*)(L + L_MISC + 8) = e1; *done_cnt = 0u; }
      ((LAS unsigned*)(L + L_A + 8 * A_STR))[lane] = 0u;
      if (lane < 5) ((LAS int*)(L + L_CTL))[lane] = (int)(pace_t >> 4) - GM_TA + GM_OFF;
      if (GM_QSLEEP) __builtin_amdgcn_s_sleep(GM_QSLEEP);
      u64 gv[8];
      gather8(qx + (size_t)(h * NB + b) * 512 + lane, tag, gv);
      ((LAS u16*)(L + L_Q))[lane] = f2bf((GVAL(gv[0]) + GVAL(gv[2])) + (GVAL(gv[4]) + GVAL(gv[6])));
      ((LAS u16*)(L + L_Q))[64 + lane] = f2bf((GVAL(gv[1]) + GVAL(gv[3])) + (GVAL(gv[5]) + GVAL(gv[7])));
    }
    WG_BARRIER();
    const GAS unsigned char* wob = (const GAS unsigned char*)(a.wo + (size_t)h * DH * DM);
    int so[9];
#pragma unroll
    for (int j = 0; j < 9; ++j) {
      const int sl = 64 * j + lane, rr = sl / 72, ck = sl % 72;
      so[j] = (8 * (rr & 3) + 2 * (rr >> 2)) * 1152 + ((((ck >> 1) ^ ((rr >> 1) & 3)) << 1) | (ck & 1)) * 16;
    }
#pragma unroll 1
    for (int gr = 0; gr < 16; ++gr) {
      const int ro = (32 * (gr >> 2) + 4 * (gr & 1) + ((gr >> 1) & 1)) * 1152;
      GAS u64* pp = (GAS u64*)(a.ws + W_PACE);
      u64 pv = 0;
      const bool rd = GM_CK && gr % GM_CK == GM_CK - 1 && gr > 1;
      if (rd) pv = __hip_atomic_load(pp + ((gr - 1) * 8 + b) * 32 + (lane & 31), RLX_AGENT);
      if (GM_CK && b == 3 && (gr + 1) % GM_CK == GM_CK - 1 && gr > 0) {
        const v4u ev = *(volatile LAS v4u*)(L + L_CTL);
        const int e0 = (int)ev[0] + ((((int)ev[1] - (int)ev[0]) + ((int)ev[2] - (int)ev[0]) + ((int)ev[3] - (int)ev[0])) >> 2);
        if (lane < 8) __hip_atomic_store(pp + (gr * 8 + lane) * 32 + h, (tag << 24) | (u64)(e0 & 0xffffff), RLX_AGENT);
      }
#pragma unroll
      for (int j = 0; j < 9; ++j) {
        PACE(GM_AD * 16 / 144); glds16<GM_WPOL != 0>(wob + ro + so[j], (unsigned)(size_t)(L + L_WO) + gr * 9216 + j * 1024); VMWAIT(GM_AW);
        if (j == 3 && rd) {
          const int old = *(volatile LAS int*)(L + L_CTL + 16);
          const float df = (float)((((int)(unsigned)pv - old) << 8) >> 8);
          const int adj = (int)(wave_sum(df) * (1.f / 64.f));
          if (__builtin_amdgcn_ballot_w64((pv >> 24) != tag) == 0 && lane == 0) *(volatile LAS int*)(L + L_CTL + 16) = old + adj - GM_L0;
        }
      }
    }
    VMWAIT(0);
  } else {
    const int m = w - 1;
    pace_t = (pace_t + (GM_TA - 40)) << 4;
    WG_BARRIER();
    const GAS u16* xp = (const GAS u16*)(a.x + b * DM);
    float xv[9];
#pragma unroll
    for (int i = 0; i < 9; ++i) xv[i] = bf2f(xp[lane + 64 * i]);
    const int col0 = 64 * ((9 * h) / 4 + m), sl = 4 * (c >> 3) + g;
    const GAS unsigned char* w1p = (const GAS unsigned char*)a.w1 + (size_t)(72 * sl) * (FF * 2) + col0 * 2 + (c & 7) * 16;
    constexpr int R1 = GM_R1;
    v4u r1[R1];
#pragma unroll
    for (int i = 0; i < R1; ++i) { r1[i] = ld_nt((const GAS v4u*)(w1p + i * (FF * 2))); SCHED_FENCE(); }
    float b1v[4];
#pragma unroll
    for (int r = 0; r < 4; ++r) b1v[r] = bf2f(((const GAS u16*)a.b1)[col0 + 8 * (4 * (g & 1) + r) + (c & 7)]);
    if (m == 0) {
      const GAS u16* gp = (const GAS u16*)a.gw;
      float gs = 0.f;
#pragma unroll
      for (int i = 0; i < 9; ++i) gs += rbf(xv[i] * bf2f(gp[lane + 64 * i]));
      const float logit = rbf(wave_sum(gs));
      const float gate = rbf(1.f / (1.f + __expf(-logit)));
      if (lane == 0) *(LAS float*)(L + L_MISC) = gate;
    }
    float sum = 0.f;
#pragma unroll
    for (int i = 0; i < 9; ++i) sum += xv[i];
    const float mean = wave_sum(sum) * (1.f / DM);
    float sq = 0.f;
#pragma unroll
    for (int i = 0; i < 9; ++i) sq += (xv[i] - mean) * (xv[i] - mean);
    const float var = rbf(wave_sum(sq) * (1.f / DM));
    const float mb = rbf(mean), sd = rbf(sqrtf(rbf(var + 1e-5f)));
    LAS u16* yl = (LAS u16*)(L + L_Y + m * 1152);
#pragma unroll
    for (int i = 0; i < 9; ++i) yl[lane + 64 * i] = f2bf(rbf(xv[i] - mb) / sd);
    LDS_WAIT();
    unsigned mask[4];
#pragma unroll
    for (int r = 0; r < 4; ++r) mask[r] = ((c & 7) >> 1) == r ? ((c & 1) ? 0xffff0000u : 0x0000ffffu) : 0u;
    f32x4 h1 = {0.f, 0.f, 0.f, 0.f};
    static_assert(24 % R1 == 0, "ring");
#pragma unroll 1
    for (int o = 0; o < 72; o += 24) {
      v4u yy[3];
#pragma unroll
      for (int i = 0; i < 3; ++i) yy[i] = *(const LAS v4u*)(yl + 72 * sl + o + 8 * i);
#pragma unroll
      for (int i = 0; i < 24; ++i) {
        const unsigned u = yy[i >> 3][(i >> 1) & 3];
        const unsigned dup = __builtin_amdgcn_perm(u, u, (i & 1) ? 0x03020302u : 0x01000100u);
        h1 = mfma(r1[i % R1], v4u{dup & mask[0], dup & mask[1], dup & mask[2], dup & mask[3]}, h1);
        SCHED_FENCE();
        const int nx = o + i + R1 < 72 ? o + i + R1 : 71;
        PACE(GM_AD * 16 / 144);
        r1[i % R1] = ld_nt((const GAS v4u*)(w1p + (size_t)nx * (FF * 2)));
        SCHED_FENCE();
      }
    }
    const int lo = col0 > FS * h ? col0 : FS * h, hi = col0 + 64 < FS * h + FS ? col0 + 64 : FS * h + FS;
    const int nrg = (hi - lo) >> 3;
    const unsigned step2 = (GM_AD * 16 / 18) / nrg;
    const GAS unsigned char* w2p = (const GAS unsigned char*)a.w2 + (size_t)(lo + sl) * (DM * 2) + (c & 7) * 16;
    v4u r2[3];
#pragma unroll
    for (int i = 0; i < 3; ++i) { r2[i] = ld_nt((const GAS v4u*)(w2p + i * 128)); SCHED_FENCE(); }
    LAS u16* hid = (LAS u16*)(L + L_HID + m * 128);
#pragma unroll
    for (int r = 0; r < 4; ++r) {
      const float pre = rbf(rbf(fold_halves(h1[r])) + b1v[r]);
      const float inner = 0.7978845608028654f * (pre + 0.044715f * pre * pre * pre);
      const float th = 1.f - 2.f / (1.f + __expf(2.f * inner));
      if (lane < 32 && c < 8) hid[8 * (4 * g + r) + c] = f2bf(0.5f * pre * (1.f + th));
    }
    LDS_WAIT();
    f32x4 o2[9];
#pragma unroll
    for (int j = 0; j < 9; ++j) o2[j] = f32x4{0.f, 0.f, 0.f, 0.f};
#pragma unroll 1
    for (int rg = 0; rg < nrg; ++rg) {
      const unsigned u = hid[lo - col0 + 8 * rg + sl];
      const unsigned dup = u | (u << 16);
      const v4u bf = {dup & mask[0], dup & mask[1], dup & mask[2], dup & mask[3]};
      const GAS unsigned char* cur = w2p + (size_t)rg * (8 * DM * 2);
      const GAS unsigned char* nxt = w2p + (size_t)(rg + 1 < nrg ? rg + 1 : rg) * (8 * DM * 2);
#pragma unroll
      for (int j = 0; j < 9; ++j) {
        o2[j] = mfma(r2[j % 3], bf, o2[j]);
        SCHED_FENCE();
        PACE(step2);
        r2[j % 3] = ld_nt((const GAS v4u*)(j < 6 ? cur + (j + 3) * 128 : nxt + (j - 6) * 128));
        SCHED_FENCE();
      }
    }
    LAS float* mp = (LAS float*)(L + L_MLP + m * 2304);
#pragma unroll
    for (int j = 0; j < 9; ++j) {
      f32x4 t;
#pragma unroll
      for (int r = 0; r < 4; ++r) t[r] = fold_halves(o2[j][r]);
      if (lane < 32 && c < 8) {
#pragma unroll
        for (int r = 0; r < 4; ++r) mp[64 * j + 8 * (4 * g + r) + c] = t[r];
      }
    }
  }
  LDS_WAIT();
  if (lane == 0) (void)__hip_atomic_fetch_add(done_cnt, 1u, __ATOMIC_RELAXED, __HIP_MEMORY_SCOPE_WORKGROUP);
  while (__builtin_amdgcn_readfirstlane(__hip_atomic_load(done_cnt, __ATOMIC_RELAXED, __HIP_MEMORY_SCOPE_WORKGROUP)) < (unsigned)(NWA + NWS)) __builtin_amdgcn_s_sleep(8);
  asm volatile("" ::: "memory");
  v4u bf[9][4];
  {
    const int q4 = (lane >> 2) & 3, p4 = lane & 3;
    LAS unsigned char* tb = L + L_WO + (4 * g + q4) * 1152 + ((w ^ ((2 * g + (q4 >> 1)) & 3)) * 32) + 8 * p4;
#pragma unroll
    for (int j = 0; j < 9; ++j)
#pragma unroll
      for (int ks = 0; ks < 4; ++ks) bf[j][ks] = tr_frag(tb + ks * 32 * 1152 + j * 128, tb + (ks * 32 + 16) * 1152 + j * 128);
  }
  const float gate = *(const LAS float*)(L + L_MISC);
  const float omg = rbf(1.f - gate);
  const u64 tag = *(const LAS u64*)(L + L_MISC + 8) & TAG_MASK;
  const int jg = 2 * (g & 1) + (g >> 1);
  float base[3];
#pragma unroll
  for (int i = 0; i < 3; ++i) {
    const int n = 16 * (w + 4 * (i < 2 ? 4 * i + jg : 8)) + c;
    const LAS float* mp = (const LAS float*)(L + L_MLP);
    base[i] = omg * ((mp[n] + mp[DM + n]) + mp[2 * DM + n]);
  }
  int ox = (((w + 4 * jg) * 32 + h) * 16 + c) * 8;
  int ox2 = g == 0 ? ox + 2 * 16 * 4096 : 36 * 4096;
  f32x4 cw;
  {
    const f32x4 ml0 = *(const LAS f32x4*)(L + L_ML), ml1 = *(const LAS f32x4*)(L + L_ML + 16);
    const float mx = fmaxf(fmaxf(ml0[0], ml0[2]), fmaxf(ml1[0], ml1[2]));
    cw = f32x4{__builtin_amdgcn_exp2f(ml0[0] - mx), __builtin_amdgcn_exp2f(ml0[2] - mx), __builtin_amdgcn_exp2f(ml1[0] - mx), __builtin_amdgcn_exp2f(ml1[2] - mx)};
    const float den = (cw[0] * ml0[1] + cw[1] * ml0[3]) + (cw[2] * ml1[1] + cw[3] * ml1[3]);
    cw *= gate / den;
  }
#pragma unroll
  for (int j = 0; j < 9; ++j) asm volatile("" : "+v"(bf[j][0]), "+v"(bf[j][1]), "+v"(bf[j][2]), "+v"(bf[j][3]));
  asm volatile("" : "+v"(base[0]), "+v"(base[1]), "+v"(base[2]), "+v"(ox), "+v"(ox2), "+v"(cw));
#pragma unroll 1
  for (int pass = GM_DRY ? 0 : 1; pass < 2; ++pass) {
    int real = pass;
    asm volatile("" : "+s"(real));
    const rsrc_t ro = __builtin_amdgcn_make_buffer_rsrc((void*)(a.ws + W_OX + (size_t)b * 36 * 4096), 0, real ? 36 * 4096 : 0, 0x00020000);
    if (real) WG_BARRIER();
    v4u af[2][4];
    LAS unsigned char* ap0 = L + L_A + (c < 8 ? c : 8) * A_STR + 16 * g;
    LAS unsigned char* ap1 = L + L_A + (c < 8 ? 8 : c - 8) * A_STR + 16 * g;
    asm volatile("ds_read_b128 %0, %8\n\tds_read_b128 %4, %9\n\tds_read_b128 %1, %8 offset:64\n\tds_read_b128 %5, %9 offset:64\n\t"
                 "ds_read_b128 %2, %8 offset:128\n\tds_read_b128 %6, %9 offset:128\n\tds_read_b128 %3, %8 offset:192\n\tds_read_b128 %7, %9 offset:192\n\t"
                 "s_waitcnt lgkmcnt(0)"
                 : "=&v"(af[0][0]), "=&v"(af[0][1]), "=&v"(af[0][2]), "=&v"(af[0][3]), "=&v"(af[1][0]), "=&v"(af[1][1]), "=&v"(af[1][2]), "=&v"(af[1][3])
                 : "v"(ap0), "v"(ap1) : "memory");
    f32x4 oc[5];
#pragma unroll
    for (int p = 0; p < 5; ++p) oc[p] = f32x4{0.f, 0.f, 0.f, 0.f};
    auto run_pair = [&](int p0) {
#pragma unroll
      for (int v = 0; v < 2; ++v)
#pragma unroll
        for (int ks = 0; ks < 4; ++ks) {
          oc[p0] = mfma(af[v][ks], bf[2 * p0 + v][ks], oc[p0]);
          oc[p0 + 1] = mfma(af[v][ks], bf[2 * p0 + 2 + v][ks], oc[p0 + 1]);
        }
    };
    auto put = [&](int i, f32x4 u, f32x4 v) {
      const float tu = fmaf(cw[3], u[3], fmaf(cw[2], u[2], fmaf(cw[1], u[1], cw[0] * u[0])));
      const float tv = fmaf(cw[3], v[3], fmaf(cw[2], v[2], fmaf(cw[1], v[1], cw[0] * v[0])));
      auto sw = __builtin_amdgcn_permlane16_swap(__float_as_uint(tu), __float_as_uint(tv), false, false);
      const u64 gr = granule(tag, (__uint_as_float(sw[0]) + __uint_as_float(sw[1])) + base[i]);
      __builtin_amdgcn_raw_buffer_store_b64(__builtin_bit_cast(v2u, gr), ro, i < 2 ? ox : ox2, i == 1 ? 16 * 4096 : 0, 16);
    };
    run_pair(0);
#pragma unroll
    for (int ks = 0; ks < 4; ++ks) oc[4] = mfma(af[0][ks], bf[8][ks], oc[4]);
    put(0, oc[0], oc[1]);
    run_pair(2);
    put(2, oc[4], oc[4]);
    put(1, oc[2], oc[3]);
  }
}

}

extern "C" void kernel_launch(void* const* d_in, const int* in_sizes, int n_in, void* d_out, int out_size, void* d_ws,
                              size_t ws_size, hipStream_t stream) {
  (void)in_sizes; (void)n_in; (void)out_size; (void)ws_size;
  gm::Args a;
  a.x = (const gm::u16*)d_in[0]; a.k = (const gm::u16*)d_in[1]; a.v = (const gm::u16*)d_in[2];
  a.wq = (const gm::u16*)d_in[3]; a.wo = (const gm::u16*)d_in[4];
  a.w1 = (const gm::u16*)d_in[6]; a.b1 = (const gm::u16*)d_in[7]; a.w2 = (const gm::u16*)d_in[8]; a.b2 = (const gm::u16*)d_in[9];
  a.gw = (const gm::u16*)d_in[10];
  a.out = (gm::u16*)d_out; a.ws = (unsigned char*)d_ws;
  a.touch[0] = gm::Touch{(const unsigned char*)a.wq, 1024u, 18u * 8192u, GM_PF == 2 ? 17u * 8192u + 1024u : 0u, 64u, 8192u - 1024u, 0u};
  a.touch[1] = gm::Touch{(const unsigned char*)a.w1, 1152u, 18u * 9216u, 17u * 9216u + 1152u, 57u, 9216u - 1152u, 0u};
  a.touch[2] = gm::Touch{(const unsigned char*)a.w2, 576u * 1152u, 18u * 1152u, 18u * 1152u, 0u, 0u, 0u};
  a.touch[3] = gm::Touch{(const unsigned char*)a.wo, 512u * 1152u, 16u * 1152u, 16u * 1152u, 0u, 0u, 0u};
  (void)hipFuncSetAttribute((const void*)gm::fused_kernel, hipFuncAttributeMaxDynamicSharedMemorySize, gm::L_BYTES);
  hipLaunchKernelGGL(gm::fused_kernel, dim3(8, 4, 8), dim3(gm::NTHR), gm::L_BYTES, stream, a);
}
```

```cpp
#include <hip/hip_runtime.h>
#include <hip/hip_bf16.h>
#include <stdint.h>
#include <type_traits>

namespace gm {

constexpr int NB = 8, NH = 32, SK = 4096, DH = 128, DM = 576, FF = 4608;
constexpr int FS = FF / NH;
constexpr int NWS = 4, NWA = 4, NTHR = (NWS + NWA) * 64;
constexpr int NS = SK / 64 / NWS;
#define GM_EXTRA 1
constexpr int EXTRA = GM_EXTRA;
#define GM_SW0 5
#define GM_AW 2
#define GM_R1 3
#define GM_TOUCH 0
#define GM_QSLEEP 40
#define GM_PAT 0
#define GM_LO 3
#define GM_CTL 1
#define GM_ROT 7
#define GM_OFF 330
#define GM_P1 1735
#define GM_P2 1735
#define GM_I1 52
#define GM_STEP 75
#define GM_TA 900
#define GM_AD 5400
#define GM_CK 3
#define GM_L0 10
#define GM_GSLEEP 24
#define GM_DRY 0
#define GM_PF 1
#define GM_WPOL 0
#define GM_T0 28
#define GM_T1 28
#define GM_T2 28
#define GM_CODE 640
#define GM_VT 1
#define GM_LAZY 1
#define GM_TT 0
#define GM_TW 0
#define GM_TP 0
#define GM_TS 600
#define STR2(x) #x
#define STR(x) STR2(x)
#define VMWAIT(n) asm volatile("s_waitcnt vmcnt(" STR(n) ")" ::: "memory")

constexpr int L_WO = 0;
constexpr int A_STR = 272;
constexpr int L_A = 128 * 1152;
constexpr int L_MLP = L_A + 9 * A_STR;
constexpr int L_Y = L_MLP + 3 * 2304;
constexpr int L_HID = L_Y + 3 * 1152;
constexpr int L_ML = L_HID + 3 * 128;
constexpr int L_Q = L_ML + 32;
constexpr int L_MISC = L_Q + 256;
constexpr int L_CTL = L_MISC + 16;
constexpr int L_BYTES = L_CTL + 32;
static_assert(L_BYTES <= 163840 && L_MLP % 16 == 0 && L_ML % 16 == 0 && L_MISC % 16 == 0, "LDS");

constexpr size_t W_EPOCH = 0;
constexpr size_t W_QX = 4096;
constexpr size_t W_PACE = 4 << 20;
constexpr size_t W_OX = W_QX + (size_t)NH * NB * 4 * 128 * 8;

#define GAS __attribute__((address_space(1)))
#define LAS __attribute__((address_space(3)))
#define CAS __attribute__((address_space(4)))
typedef unsigned v4u __attribute__((ext_vector_type(4)));
typedef unsigned v2u __attribute__((ext_vector_type(2)));
typedef unsigned v8u __attribute__((ext_vector_type(8)));
typedef float f32x4 __attribute__((ext_vector_type(4)));
typedef short s16x4 __attribute__((ext_vector_type(4)));
typedef __bf16 bf16x2 __attribute__((ext_vector_type(2)));
typedef __bf16 bf16x8 __attribute__((ext_vector_type(8)));
typedef unsigned long long u64;
typedef unsigned short u16;
typedef __amdgpu_buffer_rsrc_t rsrc_t;

struct Touch { const unsigned char* base; unsigned mx, ms, span, sb; };
struct Args {
  const u16* k; const u16* v; const u16* wq; const u16* x; unsigned char* ws;
  const u16* wo; const u16* w1; const u16* b1; const u16* w2; const u16* b2; const u16* gw; u16* out;
  Touch touch[3];
};
static_assert(sizeof(Args) == 3 * 24 + 12 * 8, "no padding");
__device__ __forceinline__ const CAS Args& args() {
  const CAS Args* p = (const CAS Args*)__builtin_amdgcn_kernarg_segment_ptr();
  asm volatile("" : "+s"(p));
  return *p;
}

__device__ __forceinline__ v4u ld_nt(const GAS v4u* p) { if (GM_WPOL) return __builtin_nontemporal_load(p); else return *p; }
__device__ __forceinline__ v4u ld_kv(rsrc_t rs, int voff, int soff) { return __builtin_amdgcn_raw_buffer_load_b128(rs, voff, soff, 2); }
template <bool NT> __device__ __forceinline__ void glds16(const GAS unsigned char* g, unsigned lds_base) {
  unsigned keep;
  if (!NT) asm volatile("s_mov_b32 %0, m0\n\ts_mov_b32 m0, %2\n\ts_nop 0\n\tglobal_load_lds_dwordx4 %1, off\n\ts_mov_b32 m0, %0" : "=&s"(keep) : "v"(g), "s"(lds_base) : "memory");
  else
  asm volatile("s_mov_b32 %0, m0\n\ts_mov_b32 m0, %2\n\ts_nop 0\n\tglobal_load_lds_dwordx4 %1, off nt\n\ts_mov_b32 m0, %0" : "=&s"(keep) : "v"(g), "s"(lds_base) : "memory");
}
__device__ __forceinline__ float bf2f(unsigned h) { return __builtin_bit_cast(float, h << 16); }
__device__ __forceinline__ float bflo(unsigned u) { return __builtin_bit_cast(float, u << 16); }
__device__ __forceinline__ float bfhi(unsigned u) { return __builtin_bit_cast(float, u & 0xffff0000u); }
__device__ __forceinline__ unsigned pk_bf16(float lo, float hi) {
  bf16x2 p = {(__bf16)lo, (__bf16)hi};
  return __builtin_bit_cast(unsigned, p);
}
__device__ __forceinline__ float rbf(float f) { return (float)(__bf16)f; }
__device__ __forceinline__ u16 f2bf(float f) { return __builtin_bit_cast(u16, (__bf16)f); }
__device__ __forceinline__ float dot2(unsigned a, unsigned b, float c) {
  return __builtin_amdgcn_fdot2_f32_bf16(__builtin_bit_cast(bf16x2, a), __builtin_bit_cast(bf16x2, b), c, false);
}
__device__ __forceinline__ f32x4 mfma(v4u a, v4u b, f32x4 c) {
  return __builtin_amdgcn_mfma_f32_16x16x32_bf16(__builtin_bit_cast(bf16x8, a), __builtin_bit_cast(bf16x8, b), c, 0, 0, 0);
}
__device__ __forceinline__ v4u tr_frag(LAS unsigned char* lo, LAS unsigned char* hi) {
  const v2u l2 = __builtin_bit_cast(v2u, __builtin_amdgcn_ds_read_tr16_b64_v4i16((LAS s16x4*)lo));
  const v2u h2 = __builtin_bit_cast(v2u, __builtin_amdgcn_ds_read_tr16_b64_v4i16((LAS s16x4*)hi));
  return v4u{l2[0], l2[1], h2[0], h2[1]};
}
template <int CTRL> __device__ __forceinline__ float dppf(float x) {
  return __builtin_bit_cast(float, __builtin_amdgcn_mov_dpp(__builtin_bit_cast(int, x), CTRL, 0xf, 0xf, true));
}
template <int CTRL> __device__ __forceinline__ unsigned dppu(unsigned x) {
  return (unsigned)__builtin_amdgcn_mov_dpp((int)x, CTRL, 0xf, 0xf, true);
}
constexpr int XOR1 = 0xB1, XOR2 = 0x4E, XOR7 = 0x141, XOR8 = 0x128, BCAST = 0x150;
__device__ __forceinline__ float x16_sum(float x) {
  auto s = __builtin_amdgcn_permlane16_swap(__float_as_uint(x), __float_as_uint(x), false, false);
  return __uint_as_float(s[0]) + __uint_as_float(s[1]);
}
__device__ __forceinline__ float xrow_sum(float x) {
  x = x16_sum(x);
  auto t = __builtin_amdgcn_permlane32_swap(__float_as_uint(x), __float_as_uint(x), false, false);
  return __uint_as_float(t[0]) + __uint_as_float(t[1]);
}
__device__ __forceinline__ float xrow_max(float x) {
  auto s = __builtin_amdgcn_permlane16_swap(__float_as_uint(x), __float_as_uint(x), false, false);
  x = fmaxf(__uint_as_float(s[0]), __uint_as_float(s[1]));
  auto t = __builtin_amdgcn_permlane32_swap(__float_as_uint(x), __float_as_uint(x), false, false);
  return fmaxf(__uint_as_float(t[0]), __uint_as_float(t[1]));
}
__device__ __forceinline__ float fold_halves(float x) {
  const float t = dppf<XOR8>(x);
  auto p = __builtin_amdgcn_permlane32_swap(__float_as_uint(t), __float_as_uint(t), false, false);
  return x + __uint_as_float(p[1]);
}
__device__ __forceinline__ float wave_sum(float x) {
  x += dppf<XOR1>(x); x += dppf<XOR2>(x); x += dppf<XOR7>(x); x += dppf<XOR8>(x);
  return xrow_sum(x);
}
__device__ __forceinline__ float wave_max(float x) {
  x = fmaxf(x, dppf<XOR1>(x)); x = fmaxf(x, dppf<XOR2>(x)); x = fmaxf(x, dppf<XOR7>(x)); x = fmaxf(x, dppf<XOR8>(x));
  return xrow_max(x);
}
#define WG_BARRIER() asm volatile("s_waitcnt lgkmcnt(0)\n\ts_barrier" ::: "memory")
#define LDS_WAIT() asm volatile("s_waitcnt lgkmcnt(0)" ::: "memory")
#define SCHED_FENCE() __builtin_amdgcn_sched_barrier(0)
#define RLX_AGENT __ATOMIC_RELAXED, __HIP_MEMORY_SCOPE_AGENT

constexpr float SCL = 0.088388347648318440f * 1.4426950408889634f;
constexpr u64 TAG_MASK = (1ull << 40) - 1;

__device__ __forceinline__ u64 granule(u64 tag, float v) { return (tag << 24) | (u64)((__builtin_bit_cast(unsigned, v) + 0x80u) >> 8); }
#define GVAL(x) __builtin_bit_cast(float, (unsigned)(x) << 8)
__device__ __forceinline__ void gather8(GAS u64* ib, u64 tag, u64 (&gv)[8]) {
#pragma unroll
  for (int i = 0; i < 8; ++i) gv[i] = __hip_atomic_load(ib + 64 * i, RLX_AGENT);
  for (unsigned spin = 0; spin < (1u << 22); ++spin) {
    bool all = true;
#pragma unroll
    for (int i = 0; i < 8; ++i) all = all && (gv[i] >> 24) == tag;
    if (__builtin_amdgcn_ballot_w64(!all) == 0) break;
    __builtin_amdgcn_s_sleep(1);
#pragma unroll
    for (int i = 0; i < 8; ++i)
      if ((gv[i] >> 24) != tag) gv[i] = __hip_atomic_load(ib + 64 * i, RLX_AGENT);
  }
}

typedef float f32x16 __attribute__((ext_vector_type(16)));
constexpr int LAG_FREE = 1 << 20;
#define THROTTLE(lag, i) asm volatile( \
    "s_cmp_ge_i32 %0, 0x100000\n\ts_cbranch_scc1 1f\n\ts_waitcnt vmcnt(%c1)\n\t" \
    "s_cmp_gt_i32 %0, %c5\n\ts_cbranch_scc1 1f\n\ts_waitcnt vmcnt(%c2)\n\t" \
    "s_cmp_gt_i32 %0, %c6\n\ts_cbranch_scc1 1f\n\ts_waitcnt vmcnt(%c3)\n\t" \
    "s_cmp_gt_i32 %0, %c7\n\ts_cbranch_scc1 1f\n\ts_waitcnt vmcnt(%c4)\n1:" \
    :: "s"(lag), "i"(GM_LO + 3), "i"(GM_LO + 2), "i"(GM_LO + 1), "i"(GM_LO), \
       "i"(GM_STEP + DITH(i)), "i"(DITH(i)), "i"(-GM_STEP + DITH(i)) : "memory", "scc")
#define DITH(i) (GM_STEP * ((((i) & 1) * 4 + ((i) & 2) + ((i) >> 2)) * 2 + 1) / 16)

template <int J> __device__ __forceinline__ void throttle(int lag) { THROTTLE(lag, (J & 7)); }

#define QUARTER_OFF(st, d) ((((st) ^ rot) << 14) + ((d) << 18))

struct Sel { bool b8, b4, b2, b1; };
struct Red { float x0, x1, x2, x3; };
template <int CTRL> __device__ __forceinline__ float merge2(float lo, float hi, bool take_hi) {
  const float u = lo + dppf<CTRL>(lo), v = hi + dppf<CTRL>(hi);
  return take_hi ? v : u;
}
template <int J> __device__ __forceinline__ void red_push(Red& r, float d, const Sel& s) {
  if (!(J & 1)) { r.x0 = d; return; }
  d = merge2<XOR8>(r.x0, d, s.b8);
  if (!(J & 2)) { r.x1 = d; return; }
  d = merge2<XOR7>(r.x1, d, s.b4);
  if (!(J & 4)) { r.x2 = d; return; }
  d = merge2<XOR2>(r.x2, d, s.b2);
  if (!(J & 8)) { r.x3 = d; return; }
  r.x0 = merge2<XOR1>(r.x3, d, s.b1);
}
__device__ __forceinline__ float dot8(const v4u k, const v4u q) {
  float d = dot2(k[0], q[0], 0.f);
  d = dot2(k[1], q[1], d); d = dot2(k[2], q[2], d);
  return dot2(k[3], q[3], d);
}
template <int J, int N> struct Unroll {
  template <typename F> static __device__ __forceinline__ void run(F&& f) { f(std::integral_constant<int, J>{}); Unroll<J + 1, N>::run(f); }
};
template <int N> struct Unroll<N, N> { template <typename F> static __device__ __forceinline__ void run(F&&) {} };

__global__ void __launch_bounds__(NTHR, 2) fused_kernel(Args) {
  extern __shared__ __attribute__((aligned(16))) unsigned char smem[];
  LAS unsigned char* L = (LAS unsigned char*)smem;
  const int tid = threadIdx.x, lane = tid & 63, w = __builtin_amdgcn_readfirstlane(tid >> 6);
  v4u kb;
  u64 pc0;
  asm volatile("s_load_dwordx4 %0, %2, 0x0\n\ts_getpc_b64 %1" : "=&s"(kb), "=&s"(pc0) : "s"(__builtin_amdgcn_kernarg_segment_ptr()) : "memory");
  const int xc = blockIdx.x, b = blockIdx.z, h = xc * 4 + blockIdx.y, sx = b * 4 + blockIdx.y;
  const int bid = sx * 8 + xc;
  const int g = lane >> 4, c = lane & 15;

  if (__builtin_expect(w >= NWA, 1)) {
    const int ws = w - NWA;
    const unsigned t_zero = GM_CK ? 0u : (unsigned)__builtin_amdgcn_s_memrealtime();
    const size_t kvoff = (size_t)(b * NH + h) * SK * DH * 2;
    const int voff = lane * 16 + ws * 4096;
    const int rot = sx & GM_ROT;
    asm volatile("s_waitcnt lgkmcnt(0)" : "+s"(kb) :: "memory");
    const unsigned char* kp = (const unsigned char*)(((u64)kb[1] << 32) | kb[0]) + kvoff;
    const unsigned char* vp = (const unsigned char*)(((u64)kb[3] << 32) | kb[2]) + kvoff;
    const rsrc_t rk = __builtin_amdgcn_make_buffer_rsrc((void*)kp, 0, SK * DH * 2, 0x00020000);
    const rsrc_t rv = __builtin_amdgcn_make_buffer_rsrc((void*)vp, 0, SK * DH * 2, 0x00020000);
    v4u ring[16], xr[EXTRA ? EXTRA : 1][16];
#define FINE(i)
#pragma unroll
    for (int e = 0; e <= EXTRA; ++e)
#pragma unroll
      for (int j = 0; j < 16; ++j) {
        if (e > 0 || j > GM_SW0) VMWAIT(GM_SW0);
        if (j % 2 == 0) FINE(e * 8 + j / 2);
        const v4u t_ = ld_kv(rk, voff + (j & 3) * 1024, QUARTER_OFF(e, j >> 2));
        if (e == 0) ring[j] = t_; else xr[e - 1][j] = t_;
        SCHED_FENCE();
      }
    const Sel sel = {(c & 8) != 0, (c & 4) != 0, (c & 2) != 0, (c & 1) != 0};
    WG_BARRIER();
    const v4u q = *(const LAS v4u*)(L + L_Q + 16 * c);
    auto lag_of = [&](int i) -> int {
      if (!GM_CTL) return 0;
      const int e = (int)(unsigned)__builtin_amdgcn_s_memrealtime() - ((GM_P2 * i + (GM_P1 - GM_P2) * (i < GM_I1 ? i : GM_I1)) >> 4);
      if (!GM_CK) return e - (int)t_zero - GM_OFF;
      const int base = *(volatile LAS int*)(L + L_CTL + 16);
      *(volatile LAS int*)(L + L_CTL + 4 * ws) = e;
      return e - __builtin_amdgcn_readfirstlane(base);
    };
    f32x16 zs;
    float mx = -INFINITY;
#pragma unroll
    for (int e = 0; e < EXTRA; ++e) {
      Red ra, rb;
      int lag = 0;
      Unroll<0, 16>::run([&](auto jc) {
        constexpr int j = decltype(jc)::value;
        if (j % 8 == 0) lag = lag_of(2 * (EXTRA + 1 + e) + j / 8);
        float d = dot8(ring[j], q);
        asm volatile("" : "+v"(d));
        throttle<j>(lag);
        ring[j] = ld_kv(rk, voff + (j & 3) * 1024, QUARTER_OFF(EXTRA + 1 + e, j >> 2));
        SCHED_FENCE();
        red_push<j>(ra, d, sel);
        red_push<j>(rb, dot8(xr[e][j], q), sel);
        SCHED_FENCE();
      });
      zs[e == 0 ? 0 : EXTRA + e] = ra.x0; zs[1 + e] = rb.x0;
      mx = fmaxf(mx, fmaxf(ra.x0, rb.x0));
      FINE(30 + EXTRA + 1 + e);
    }
#pragma unroll 1
    for (int n = 2 * EXTRA + 1; n <= NS; ++n) {
      const rsrc_t rs = __builtin_amdgcn_make_buffer_rsrc((void*)(n < NS ? kp : vp), 0, SK * DH * 2, 0x00020000);
      const int st = n & (NS - 1);
      Red ra;
      int lag = 0;
      Unroll<0, 16>::run([&](auto jc) {
        constexpr int j = decltype(jc)::value;
        if (j % 8 == 0) lag = lag_of(2 * n + j / 8);
        float d = dot8(ring[j], q);
        asm volatile("" : "+v"(d));
        throttle<j>(lag);
        ring[j] = ld_kv(rs, voff + (j & 3) * 1024, QUARTER_OFF(st, j >> 2));
        SCHED_FENCE();
        red_push<j>(ra, d, sel);
        SCHED_FENCE();
      });
      zs[n - 1] = ra.x0;
      mx = fmaxf(mx, ra.x0);
      FINE(30 + n);
    }
    const float mc = wave_max(mx) * SCL;
    unsigned mask[4];
#pragma unroll
    for (int r = 0; r < 4; ++r) mask[r] = (c >> 1) == r ? ((c & 1) ? 0xffff0000u : 0x0000ffffu) : 0u;
    float lp = 0.f;
    f32x4 acc = {0.f, 0.f, 0.f, 0.f};
#pragma unroll 1
    for (int n = 1; n <= NS; ++n) {
      if (n <= NS / 2) {
#pragma unroll
        for (int i = 0; i < 2; ++i) {
          const float p = __builtin_amdgcn_exp2f(fmaf(zs[2 * n - 2 + i], SCL, -mc));
          lp += p;
          zs[2 * n - 2 + i] = __uint_as_float(pk_bf16(p, p));
        }
        if (n == NS / 2) {
          const float lsum = wave_sum(lp);
          if (lane == 0) {
            volatile LAS float* ml = (volatile LAS float*)(L + L_ML);
            ml[2 * ws] = mc; ml[2 * ws + 1] = lsum;
            LDS_WAIT();
            (void)__hip_atomic_fetch_add((LAS unsigned*)(L + L_MISC + 4), 1u, __ATOMIC_RELAXED, __HIP_MEMORY_SCOPE_WORKGROUP);
          }
        }
      }
      const float pw = zs[n - 1];
      const unsigned pk = __float_as_uint(pw);
      const int oob = n < NS ? 0 : (1 << 24);
      int lag = 0;
      Unroll<0, 16>::run([&](auto jc) {
        constexpr int j = decltype(jc)::value;
        constexpr int src = ((j & 1) << 3) | ((j & 2) << 1) | ((j & 4) >> 1) | ((j & 8) >> 3);
        if (j % 8 == 0) { lag = lag_of(2 * (NS + n) + j / 8); lag = n < NS ? lag : LAG_FREE; }
        const v4u bf = {dppu<BCAST + src>(pk) & mask[0], dppu<BCAST + src>(pk) & mask[1], dppu<BCAST + src>(pk) & mask[2], dppu<BCAST + src>(pk) & mask[3]};
        acc = mfma(ring[j], bf, acc);
        asm volatile("" : "+v"(acc));
        SCHED_FENCE();
        throttle<j>(lag);
        ring[j] = ld_kv(rv, voff + (j & 3) * 1024, QUARTER_OFF(n & (NS - 1), j >> 2) + oob);
        SCHED_FENCE();
      });
      FINE(30 + NS + n);
    }
    if (c < 8) {
      const unsigned h0 = pk_bf16(acc[0], acc[1]), h1 = pk_bf16(acc[2], acc[3]);
      const unsigned l0 = pk_bf16(acc[0] - bflo(h0), acc[1] - bfhi(h0)), l1 = pk_bf16(acc[2] - bflo(h1), acc[3] - bfhi(h1));
      *(LAS v2u*)(L + L_A + ws * A_STR + 64 * g + 8 * c) = v2u{h0, h1};
      *(LAS v2u*)(L + L_A + (4 + ws) * A_STR + 64 * g + 8 * c) = v2u{l0, l1};
    }
    WG_BARRIER();
    const CAS Args& a = args();
    if (ws == 0 || (ws == 1 && h < 4)) {
      const int cb = h + 32 * ws;
      const float b2v = bf2f(((const GAS u16*)a.b2)[16 * cb + c]);
      const float omg = rbf(1.f - *(const LAS float*)(L + L_MISC));
      const u64 ep1 = *(const LAS u64*)(L + L_MISC + 8);
      const u64 tg = ep1 & TAG_MASK;
      GAS u64* ib = (GAS u64*)(a.ws + W_OX) + (size_t)(b * 36 + cb) * 512 + lane;
      GAS u16* op = (GAS u16*)a.out + b * DM + 16 * cb + c;
      u64 gv[8];
#pragma unroll
      for (int i = 0; i < 8; ++i) gv[i] = ~tg << 24;
      u16 res;
      for (unsigned spin = 0;; ++spin) {
        bool all = true;
        float sum = 0.f;
#pragma unroll
        for (int i = 0; i < 8; ++i) { all = all && (gv[i] >> 24) == tg; sum += GVAL(gv[i]); }
        res = f2bf(xrow_sum(sum) + omg * b2v);
        asm volatile("" : "+v"(res));
        if (__builtin_amdgcn_ballot_w64(!all) == 0 || spin > (1u << 22)) break;
        if (spin == 0) __builtin_amdgcn_s_sleep(GM_GSLEEP); else __builtin_amdgcn_s_sleep(1);
#pragma unroll
        for (int i = 0; i < 8; ++i)
          if ((gv[i] >> 24) != tg) gv[i] = __hip_atomic_load(ib + 64 * i, RLX_AGENT);
      }
      if (lane < 16) *op = res;
      if (ws == 0 && bid == 0 && lane == 0) __hip_atomic_store((GAS u64*)(a.ws + W_EPOCH), ep1, RLX_AGENT);
    }
    return;
  }

  asm volatile("s_waitcnt lgkmcnt(0)" :: "s"(kb) : "memory");
  __builtin_amdgcn_s_setprio(3);
  const CAS Args& a = args();
  LAS unsigned* done_cnt = (LAS unsigned*)(L + L_MISC + 4);
  u64 pace_t = __builtin_amdgcn_s_memrealtime();
#define PACE(step) do { pace_t += (step); while ((int)(((unsigned)__builtin_amdgcn_s_memrealtime() << 4) - (unsigned)pace_t) < 0) __builtin_amdgcn_s_sleep(2); } while (0)
  if (__builtin_expect(w == 0, 1)) {
    const int ch = b & 1, kq = b >> 1;
    {
      const GAS unsigned char* wqb = (const GAS unsigned char*)a.wq + (size_t)(144 * kq + 8 * (lane >> 5) + ((lane >> 3) & 3)) * (NH * DH * 2)
                                     + h * 256 + ch * 128 + (((((lane & 7) >> 1) ^ g) << 1) | (lane & 1)) * 16;
      unsigned dst = (unsigned)(size_t)(L + L_WO);
#pragma unroll 1
      for (int i = 0; i < 9; ++i) {
        glds16<true>(wqb, dst);
        glds16<true>(wqb + 4 * (NH * DH * 2), dst + 1024);
        wqb += 16 * (NH * DH * 2); dst += 2048;
      }
    }
    asm volatile("" : "+s"(pace_t));
    pace_t = (pace_t + (GM_TA - 40)) << 4;
    u64 e1 = *(const CAS u64*)(a.ws + W_EPOCH);
    const GAS unsigned char* xb = (const GAS unsigned char*)a.x + (c & 7) * (DM * 2) + 288 * kq;
    v4u xa[5];
#pragma unroll
    for (int ks = 0; ks < 4; ++ks) xa[ks] = *(const GAS v4u*)(xb + 64 * ks + 16 * g);
    xa[4] = *(const GAS v4u*)(xb + 256 + 16 * (g & 1));
    asm volatile("s_waitcnt vmcnt(0)" : "+v"(xa[0]), "+v"(xa[1]), "+v"(xa[2]), "+v"(xa[3]), "+v"(xa[4]) :: "memory");
    if (g >= 2) xa[4] = v4u{0u, 0u, 0u, 0u};
    asm volatile("" : "+s"(e1));
    e1 += 1;
    const u64 tag = e1 & TAG_MASK;
    const int q4 = (lane >> 2) & 3, p4 = lane & 3;
    {
      const int sw = 2 * (g & 1) + (q4 >> 1), rb = (4 * (g & 1) + q4) * 128 + 8 * p4;
      GAS u64* qx = (GAS u64*)(a.ws + W_QX);
      GAS u64* qo = qx + ((size_t)(h * NB + 4 * (g & 1)) * 4 + kq) * 128 + 64 * ch + 16 * (g >> 1) + c;
#pragma unroll 1
      for (int pr = 0; pr < 2; ++pr) {
        f32x4 qc[2];
#pragma unroll
        for (int j = 0; j < 2; ++j) {
          qc[j] = f32x4{0.f, 0.f, 0.f, 0.f};
          LAS unsigned char* tb = L + L_WO + rb + (((2 * pr + j) ^ sw) * 32);
#pragma unroll
          for (int ks = 0; ks < 5; ++ks) {
            LAS unsigned char* t2 = tb + ks * 4096 + (ks < 4 ? (g >> 1) * 2048 : 0);
            qc[j] = mfma(xa[ks], tr_frag(t2, t2 + 1024), qc[j]);
          }
        }
#pragma unroll
        for (int r = 0; r < 4; ++r) __hip_atomic_store(qo + r * 512 + 32 * pr, granule(tag, g < 2 ? qc[0][r] : qc[1][r]), RLX_AGENT);
      }
      if (lane == 0) { *(LAS u64*)(L + L_MISC + 8) = e1; *done_cnt = 0u; }
      ((LAS unsigned*)(L + L_A + 8 * A_STR))[lane] = 0u;
      if (lane < 5) ((LAS int*)(L + L_CTL))[lane] = (int)(pace_t >> 4) - GM_TA + GM_OFF;
      if (GM_QSLEEP) __builtin_amdgcn_s_sleep(GM_QSLEEP);
      u64 gv[8];
      gather8(qx + (size_t)(h * NB + b) * 512 + lane, tag, gv);
      ((LAS u16*)(L + L_Q))[lane] = f2bf((GVAL(gv[0]) + GVAL(gv[2])) + (GVAL(gv[4]) + GVAL(gv[6])));
      ((LAS u16*)(L + L_Q))[64 + lane] = f2bf((GVAL(gv[1]) + GVAL(gv[3])) + (GVAL(gv[5]) + GVAL(gv[7])));
    }
    WG_BARRIER();
    const GAS unsigned char* wob = (const GAS unsigned char*)(a.wo + (size_t)h * DH * DM);
    int so[9];
#pragma unroll
    for (int j = 0; j < 9; ++j) {
      const int sl = 64 * j + lane, rr = sl / 72, ck = sl % 72;
      so[j] = (8 * (rr & 3) + 2 * (rr >> 2)) * 1152 + ((((ck >> 1) ^ ((rr >> 1) & 3)) << 1) | (ck & 1)) * 16;
    }
#pragma unroll 1
    for (int gr = 0; gr < 16; ++gr) {
      const int ro = (32 * (gr >> 2) + 4 * (gr & 1) + ((gr >> 1) & 1)) * 1152;
      GAS u64* pp = (GAS u64*)(a.ws + W_PACE);
      u64 pv = 0;
      const bool rd = GM_CK && gr % GM_CK == GM_CK - 1 && gr > 1;
      if (rd) pv = __hip_atomic_load(pp + ((gr - 1) * 8 + b) * 32 + (lane & 31), RLX_AGENT);
      if (GM_CK && b == 3 && (gr + 1) % GM_CK == GM_CK - 1 && gr > 0) {
        const v4u ev = *(volatile LAS v4u*)(L + L_CTL);
        const int e0 = (int)ev[0] + ((((int)ev[1] - (int)ev[0]) + ((int)ev[2] - (int)ev[0]) + ((int)ev[3] - (int)ev[0])) >> 2);
        if (lane < 8) __hip_atomic_store(pp + (gr * 8 + lane) * 32 + h, (tag << 24) | (u64)(e0 & 0xffffff), RLX_AGENT);
      }
#pragma unroll
      for (int j = 0; j < 9; ++j) {
        PACE(GM_AD * 16 / 144); glds16<GM_WPOL != 0>(wob + ro + so[j], (unsigned)(size_t)(L + L_WO) + gr * 9216 + j * 1024); VMWAIT(GM_AW);
        if (j == 3 && rd) {
          const int old = *(volatile LAS int*)(L + L_CTL + 16);
          const float df = (float)((((int)(unsigned)pv - old) << 8) >> 8);
          const int adj = (int)(wave_sum(df) * (1.f / 64.f));
          if (__builtin_amdgcn_ballot_w64((pv >> 24) != tag) == 0 && lane == 0) *(volatile LAS int*)(L + L_CTL + 16) = old + adj - GM_L0;
        }
      }
    }
    VMWAIT(0);
  } else {
    const int m = w - 1;
    pace_t = (pace_t + (GM_TA - 40)) << 4;
    {
      const rsrc_t rc = __builtin_amdgcn_make_buffer_rsrc(m == 0 ? (void*)(pc0 + sx * GM_CODE) : (void*)(a.v + (size_t)(b * NH + h) * SK * DH), 0,
                                                          m == 0 ? GM_CODE : m == 1 && GM_VT ? 4 : 0, 0x00020000);
      const unsigned t0 = __builtin_amdgcn_raw_buffer_load_b32(rc, lane * 128, 0, 0);
      const CAS Touch& t = a.touch[m];
      const rsrc_t rp = __builtin_amdgcn_make_buffer_rsrc((void*)(t.base + (xc * t.mx + sx * t.ms)), 0, t.span, 0x00020000);
      unsigned po[3], pf[3];
#pragma unroll
      for (int i = 0; i < 3; ++i) { const unsigned id = 64 * i + lane; po[i] = id * 128 + ((id * 57) >> 9) * t.sb; }
      asm volatile("" : "+v"(po[0]), "+v"(po[1]), "+v"(po[2]) : "v"(t0));
      if (GM_TT) { while ((int)(((unsigned)__builtin_amdgcn_s_memrealtime() << 4) - ((unsigned)pace_t - (GM_TA - GM_TT) * 16)) < 0) __builtin_amdgcn_s_sleep(2); }
      else WG_BARRIER();
#pragma unroll
      for (int i = 0; i < 3; ++i) {
        if (GM_TP) { while ((int)(((unsigned)__builtin_amdgcn_s_memrealtime() << 4) - ((unsigned)pace_t - (GM_TA - GM_TS - (3 * i + m) * GM_TP) * 16)) < 0) __builtin_amdgcn_s_sleep(2); }
        pf[i] = __builtin_amdgcn_raw_buffer_load_b32(rp, po[i], 0, 0); if (GM_TW) VMWAIT(0); SCHED_FENCE(); }
      SCHED_FENCE();
      if (GM_TT) WG_BARRIER();
      asm volatile("" :: "v"(pf[0]), "v"(pf[1]), "v"(pf[2]));
    }
    const GAS u16* xp = (const GAS u16*)(a.x + b * DM);
    float xv[9];
#pragma unroll
    for (int i = 0; i < 9; ++i) xv[i] = bf2f(xp[lane + 64 * i]);
    const int col0 = 64 * ((9 * h) / 4 + m), sl = 4 * (c >> 3) + g;
    const GAS unsigned char* w1p = (const GAS unsigned char*)a.w1 + (size_t)(72 * sl) * (FF * 2) + col0 * 2 + (c & 7) * 16;
    constexpr int R1 = GM_R1;
    v4u r1[R1];
#pragma unroll
    for (int i = 0; i < R1; ++i) { r1[i] = ld_nt((const GAS v4u*)(w1p + i * (FF * 2))); SCHED_FENCE(); }
    float b1v[4];
#pragma unroll
    for (int r = 0; r < 4; ++r) b1v[r] = bf2f(((const GAS u16*)a.b1)[col0 + 8 * (4 * (g & 1) + r) + (c & 7)]);
    if (m == 0) {
      const GAS u16* gp = (const GAS u16*)a.gw;
      float gs = 0.f;
#pragma unroll
      for (int i = 0; i < 9; ++i) gs += rbf(xv[i] * bf2f(gp[lane + 64 * i]));
      const float logit = rbf(wave_sum(gs));
      const float gate = rbf(1.f / (1.f + __expf(-logit)));
      if (lane == 0) *(LAS float*)(L + L_MISC) = gate;
    }
    float sum = 0.f;
#pragma unroll
    for (int i = 0; i < 9; ++i) sum += xv[i];
    const float mean = wave_sum(sum) * (1.f / DM);
    float sq = 0.f;
#pragma unroll
    for (int i = 0; i < 9; ++i) sq += (xv[i] - mean) * (xv[i] - mean);
    const float var = rbf(wave_sum(sq) * (1.f / DM));
    const float mb = rbf(mean), sd = rbf(sqrtf(rbf(var + 1e-5f)));
    LAS u16* yl = (LAS u16*)(L + L_Y + m * 1152);
#pragma unroll
    for (int i = 0; i < 9; ++i) yl[lane + 64 * i] = f2bf(rbf(xv[i] - mb) / sd);
    LDS_WAIT();
    unsigned mask[4];
#pragma unroll
    for (int r = 0; r < 4; ++r) mask[r] = ((c & 7) >> 1) == r ? ((c & 1) ? 0xffff0000u : 0x0000ffffu) : 0u;
    f32x4 h1 = {0.f, 0.f, 0.f, 0.f};
    static_assert(24 % R1 == 0, "ring");
#pragma unroll 1
    for (int o = 0; o < 72; o += 24) {
      v4u yy[3];
#pragma unroll
      for (int i = 0; i < 3; ++i) yy[i] = *(const LAS v4u*)(yl + 72 * sl + o + 8 * i);
#pragma unroll
      for (int i = 0; i < 24; ++i) {
        const unsigned u = yy[i >> 3][(i >> 1) & 3];
        const unsigned dup = __builtin_amdgcn_perm(u, u, (i & 1) ? 0x03020302u : 0x01000100u);
        h1 = mfma(r1[i % R1], v4u{dup & mask[0], dup & mask[1], dup & mask[2], dup & mask[3]}, h1);
        SCHED_FENCE();
        const int nx = o + i + R1 < 72 ? o + i + R1 : 71;
        PACE(GM_AD * 16 / 144);
        r1[i % R1] = ld_nt((const GAS v4u*)(w1p + (size_t)nx * (FF * 2)));
        SCHED_FENCE();
      }
    }
    const int lo = col0 > FS * h ? col0 : FS * h, hi = col0 + 64 < FS * h + FS ? col0 + 64 : FS * h + FS;
    const int nrg = (hi - lo) >> 3;
    const unsigned step2 = (GM_AD * 16 / 18) / nrg;
    const GAS unsigned char* w2p = (const GAS unsigned char*)a.w2 + (size_t)(lo + sl) * (DM * 2) + (c & 7) * 16;
    v4u r2[3];
#pragma unroll
    for (int i = 0; i < 3; ++i) { r2[i] = ld_nt((const GAS v4u*)(w2p + i * 128)); SCHED_FENCE(); }
    LAS u16* hid = (LAS u16*)(L + L_HID + m * 128);
#pragma unroll
    for (int r = 0; r < 4; ++r) {
      const float pre = rbf(rbf(fold_halves(h1[r])) + b1v[r]);
      const float inner = 0.7978845608028654f * (pre + 0.044715f * pre * pre * pre);
      const float th = 1.f - 2.f / (1.f + __expf(2.f * inner));
      if (lane < 32 && c < 8) hid[8 * (4 * g + r) + c] = f2bf(0.5f * pre * (1.f + th));
    }
    LDS_WAIT();
    f32x4 o2[9];
#pragma unroll
    for (int j = 0; j < 9; ++j) o2[j] = f32x4{0.f, 0.f, 0.f, 0.f};
#pragma unroll 1
    for (int rg = 0; rg < nrg; ++rg) {
      const unsigned u = hid[lo - col0 + 8 * rg + sl];
      const unsigned dup = u | (u << 16);
      const v4u bf = {dup & mask[0], dup & mask[1], dup & mask[2], dup & mask[3]};
      const GAS unsigned char* cur = w2p + (size_t)rg * (8 * DM * 2);
      const GAS unsigned char* nxt = w2p + (size_t)(rg + 1 < nrg ? rg + 1 : rg) * (8 * DM * 2);
#pragma unroll
      for (int j = 0; j < 9; ++j) {
        o2[j] = mfma(r2[j % 3], bf, o2[j]);
        SCHED_FENCE();
        PACE(step2);
        r2[j % 3] = ld_nt((const GAS v4u*)(j < 6 ? cur + (j + 3) * 128 : nxt + (j - 6) * 128));
        SCHED_FENCE();
      }
    }
    LAS float* mp = (LAS float*)(L + L_MLP + m * 2304);
#pragma unroll
    for (int j = 0; j < 9; ++j) {
      f32x4 t;
#pragma unroll
      for (int r = 0; r < 4; ++r) t[r] = fold_halves(o2[j][r]);
      if (lane < 32 && c < 8) {
#pragma unroll
        for (int r = 0; r < 4; ++r) mp[64 * j + 8 * (4 * g + r) + c] = t[r];
      }
    }
  }
  LDS_WAIT();
  if (lane == 0) (void)__hip_atomic_fetch_add(done_cnt, 1u, __ATOMIC_RELAXED, __HIP_MEMORY_SCOPE_WORKGROUP);
  while (__builtin_amdgcn_readfirstlane(__hip_atomic_load(done_cnt, __ATOMIC_RELAXED, __HIP_MEMORY_SCOPE_WORKGROUP)) < (unsigned)(NWA + NWS)) __builtin_amdgcn_s_sleep(8);
  asm volatile("" ::: "memory");
  v4u bf[9][4];
  {
    const int q4 = (lane >> 2) & 3, p4 = lane & 3;
    LAS unsigned char* tb = L + L_WO + (4 * g + q4) * 1152 + ((w ^ ((2 * g + (q4 >> 1)) & 3)) * 32) + 8 * p4;
#pragma unroll
    for (int j = 0; j < 9; ++j)
#pragma unroll
      for (int ks = 0; ks < 4; ++ks) bf[j][ks] = tr_frag(tb + ks * 32 * 1152 + j * 128, tb + (ks * 32 + 16) * 1152 + j * 128);
  }
  const float gate = *(const LAS float*)(L + L_MISC);
  const float omg = rbf(1.f - gate);
  const u64 tag = *(const LAS u64*)(L + L_MISC + 8) & TAG_MASK;
  const int jg = 2 * (g & 1) + (g >> 1);
  float base[3];
#pragma unroll
  for (int i = 0; i < 3; ++i) {
    const int n = 16 * (w + 4 * (i < 2 ? 4 * i + jg : 8)) + c;
    const LAS float* mp = (const LAS float*)(L + L_MLP);
    base[i] = omg * ((mp[n] + mp[DM + n]) + mp[2 * DM + n]);
  }
  int ox = (((w + 4 * jg) * 32 + h) * 16 + c) * 8;
  int ox2 = g == 0 ? ox + 2 * 16 * 4096 : 36 * 4096;
  f32x4 cw;
  {
    const f32x4 ml0 = *(const LAS f32x4*)(L + L_ML), ml1 = *(const LAS f32x4*)(L + L_ML + 16);
    const float mx = fmaxf(fmaxf(ml0[0], ml0[2]), fmaxf(ml1[0], ml1[2]));
    cw = f32x4{__builtin_amdgcn_exp2f(ml0[0] - mx), __builtin_amdgcn_exp2f(ml0[2] - mx), __builtin_amdgcn_exp2f(ml1[0] - mx), __builtin_amdgcn_exp2f(ml1[2] - mx)};
    const float den = (cw[0] * ml0[1] + cw[1] * ml0[3]) + (cw[2] * ml1[1] + cw[3] * ml1[3]);
    cw *= gate / den;
  }
#pragma unroll
  for (int j = 0; j < 9; ++j) asm volatile("" : "+v"(bf[j][0]), "+v"(bf[j][1]), "+v"(bf[j][2]), "+v"(bf[j][3]));
  asm volatile("" : "+v"(base[0]), "+v"(base[1]), "+v"(base[2]), "+v"(ox), "+v"(ox2), "+v"(cw));
#pragma unroll 1
  for (int pass = GM_DRY ? 0 : 1; pass < 2; ++pass) {
    int real = pass;
    asm volatile("" : "+s"(real));
    const rsrc_t ro = __builtin_amdgcn_make_buffer_rsrc((void*)(a.ws + W_OX + (size_t)b * 36 * 4096), 0, real ? 36 * 4096 : 0, 0x00020000);
    if (real) WG_BARRIER();
    v4u af[2][4];
    LAS unsigned char* ap0 = L + L_A + (c < 8 ? c : 8) * A_STR + 16 * g;
    LAS unsigned char* ap1 = L + L_A + (c < 8 ? 8 : c - 8) * A_STR + 16 * g;
    asm volatile("ds_read_b128 %0, %8\n\tds_read_b128 %4, %9\n\tds_read_b128 %1, %8 offset:64\n\tds_read_b128 %5, %9 offset:64\n\t"
                 "ds_read_b128 %2, %8 offset:128\n\tds_read_b128 %6, %9 offset:128\n\tds_read_b128 %3, %8 offset:192\n\tds_read_b128 %7, %9 offset:192\n\t"
                 "s_waitcnt lgkmcnt(0)"
                 : "=&v"(af[0][0]), "=&v"(af[0][1]), "=&v"(af[0][2]), "=&v"(af[0][3]), "=&v"(af[1][0]), "=&v"(af[1][1]), "=&v"(af[1][2]), "=&v"(af[1][3])
                 : "v"(ap0), "v"(ap1) : "memory");
    f32x4 oc[5];
#pragma unroll
    for (int p = 0; p < 5; ++p) oc[p] = f32x4{0.f, 0.f, 0.f, 0.f};
    auto run_pair = [&](int p0) {
#pragma unroll
      for (int v = 0; v < 2; ++v)
#pragma unroll
        for (int ks = 0; ks < 4; ++ks) {
          oc[p0] = mfma(af[v][ks], bf[2 * p0 + v][ks], oc[p0]);
          oc[p0 + 1] = mfma(af[v][ks], bf[2 * p0 + 2 + v][ks], oc[p0 + 1]);
        }
    };
    auto put = [&](int i, f32x4 u, f32x4 v) {
      const float tu = fmaf(cw[3], u[3], fmaf(cw[2], u[2], fmaf(cw[1], u[1], cw[0] * u[0])));
      const float tv = fmaf(cw[3], v[3], fmaf(cw[2], v[2], fmaf(cw[1], v[1], cw[0] * v[0])));
      auto sw = __builtin_amdgcn_permlane16_swap(__float_as_uint(tu), __float_as_uint(tv), false, false);
      const u64 gr = granule(tag, (__uint_as_float(sw[0]) + __uint_as_float(sw[1])) + base[i]);
      __builtin_amdgcn_raw_buffer_store_b64(__builtin_bit_cast(v2u, gr), ro, i < 2 ? ox : ox2, i == 1 ? 16 * 4096 : 0, 16);
    };
    run_pair(0);
#pragma unroll
    for (int ks = 0; ks < 4; ++ks) oc[4] = mfma(af[0][ks], bf[8][ks], oc[4]);
    put(0, oc[0], oc[1]);
    run_pair(2);
    put(2, oc[4], oc[4]);
    put(1, oc[2], oc[3]);
  }
}

}

extern "C" void kernel_launch(void* const* d_in, const int* in_sizes, int n_in, void* d_out, int out_size, void* d_ws,
                              size_t ws_size, hipStream_t stream) {
  (void)in_sizes; (void)n_in; (void)out_size; (void)ws_size;
  gm::Args a;
  a.x = (const gm::u16*)d_in[0]; a.k = (const gm::u16*)d_in[1]; a.v = (const gm::u16*)d_in[2];
  a.wq = (const gm::u16*)d_in[3]; a.wo = (const gm::u16*)d_in[4];
  a.w1 = (const gm::u16*)d_in[6]; a.b1 = (const gm::u16*)d_in[7]; a.w2 = (const gm::u16*)d_in[8]; a.b2 = (const gm::u16*)d_in[9];
  a.gw = (const gm::u16*)d_in[10];
  a.out = (gm::u16*)d_out; a.ws = (unsigned char*)d_ws;
  a.touch[0] = gm::Touch{(const unsigned char*)a.w1, 1152u, 18u * 9216u, 17u * 9216u + 1152u, 9216u - 1152u};
  a.touch[1] = gm::Touch{(const unsigned char*)a.w2, 576u * 1152u, 18u * 1152u, 18u * 1152u, 0u};
  a.touch[2] = gm::Touch{(const unsigned char*)a.wo, 512u * 1152u, 16u * 1152u, 16u * 1152u, 0u};
  (void)hipFuncSetAttribute((const void*)gm::fused_kernel, hipFuncAttributeMaxDynamicSharedMemorySize, gm::L_BYTES);
  hipLaunchKernelGGL(gm::fused_kernel, dim3(8, 4, 8), dim3(gm::NTHR), gm::L_BYTES, stream, a);
}
```

```cpp
#include <hip/hip_runtime.h>
#include <hip/hip_bf16.h>
#include <stdint.h>
#include <type_traits>

namespace gm {

constexpr int NB = 8, NH = 32, SK = 4096, DH = 128, DM = 576, FF = 4608;
constexpr int FS = FF / NH;
constexpr int NWS = 4, NWA = 4, NTHR = (NWS + NWA) * 64;
constexpr int NS = SK / 64 / NWS;
#define GM_EXTRA 1
constexpr int EXTRA = GM_EXTRA;
#define GM_SW0 5
#define GM_AW 2
#define GM_R1 3
#define GM_TOUCH 0
#define GM_QSLEEP 40
#define GM_PAT 0
#define GM_LO 3
#define GM_CTL 1
#define GM_ROT 7
#define GM_OFF 330
#define GM_P1 1722
#define GM_P2 1722
#define GM_I1 52
#define GM_STEP 75
#define GM_TA 900
#define GM_AD 5400
#define GM_CK 3
#define GM_L0 15
#define GM_GSLEEP 20
#define GM_DRY 0
#define GM_PF 1
#define GM_WPOL 0
#define GM_T0 28
#define GM_T1 28
#define GM_T2 28
#define GM_CODE 640
#define GM_VT 1
#define GM_LAZY 1
#define GM_TT 0
#define GM_TW 0
#define GM_TP 0
#define GM_TS 600
#define GM_OST 0
#define GM_PROJ 1
#define GM_PT 0
#define GM_GT 0
#define GM_GS2 1
#define GM_TAUX 0
#define GM_W1C 1
#define GM_GX 99
#define GM_GC 25
#define STR2(x) #x
#define STR(x) STR2(x)
#define VMWAIT(n) asm volatile("s_waitcnt vmcnt(" STR(n) ")" ::: "memory")

constexpr int L_WO = 0;
constexpr int A_STR = 272;
constexpr int L_A = 128 * 1152;
constexpr int L_MLP = L_A + 9 * A_STR;
constexpr int L_Y = L_MLP + 3 * 2304;
constexpr int L_HID = L_Y + 3 * 1152;
constexpr int L_ML = L_HID + 3 * 128;
constexpr int L_Q = L_ML + 32;
constexpr int L_MISC = L_Q + 256;
constexpr int L_CTL = L_MISC + 16;
constexpr int L_BYTES = L_CTL + 32;
static_assert(L_BYTES <= 163840 && L_MLP % 16 == 0 && L_ML % 16 == 0 && L_MISC % 16 == 0, "LDS");

constexpr size_t W_EPOCH = 0;
constexpr size_t W_QX = 4096;
constexpr size_t W_PACE = 4 << 20;
constexpr size_t W_OX = W_QX + (size_t)NH * NB * 4 * 128 * 8;

#define GAS __attribute__((address_space(1)))
#define LAS __attribute__((address_space(3)))
#define CAS __attribute__((address_space(4)))
typedef unsigned v4u __attribute__((ext_vector_type(4)));
typedef unsigned v2u __attribute__((ext_vector_type(2)));
typedef unsigned v8u __attribute__((ext_vector_type(8)));
typedef float f32x4 __attribute__((ext_vector_type(4)));
typedef short s16x4 __attribute__((ext_vector_type(4)));
typedef __bf16 bf16x2 __attribute__((ext_vector_type(2)));
typedef __bf16 bf16x8 __attribute__((ext_vector_type(8)));
typedef unsigned long long u64;
typedef unsigned short u16;
typedef __amdgpu_buffer_rsrc_t rsrc_t;

struct Touch { const unsigned char* base; unsigned mx, ms, span, sb; };
struct Args {
  const u16* k; const u16* v; const u16* wq; const u16* x; unsigned char* ws;
  const u16* wo; const u16* w1; const u16* b1; const u16* w2; const u16* b2; const u16* gw; u16* out;
  Touch touch[3];
};
static_assert(sizeof(Args) == 3 * 24 + 12 * 8, "no padding");
__device__ __forceinline__ const CAS Args& args() {
  const CAS Args* p = (const CAS Args*)__builtin_amdgcn_kernarg_segment_ptr();
  asm volatile("" : "+s"(p));
  return *p;
}

__device__ __forceinline__ v4u ld_nt(const GAS v4u* p) { if (GM_WPOL) return __builtin_nontemporal_load(p); else return *p; }
__device__ __forceinline__ v4u ld_kv(rsrc_t rs, int voff, int soff) { return __builtin_amdgcn_raw_buffer_load_b128(rs, voff, soff, 2); }
template <bool NT> __device__ __forceinline__ void glds16(const GAS unsigned char* g, unsigned lds_base) {
  unsigned keep;
  if (!NT) asm volatile("s_mov_b32 %0, m0\n\ts_mov_b32 m0, %2\n\ts_nop 0\n\tglobal_load_lds_dwordx4 %1, off\n\ts_mov_b32 m0, %0" : "=&s"(keep) : "v"(g), "s"(lds_base) : "memory");
  else
  asm volatile("s_mov_b32 %0, m0\n\ts_mov_b32 m0, %2\n\ts_nop 0\n\tglobal_load_lds_dwordx4 %1, off nt\n\ts_mov_b32 m0, %0" : "=&s"(keep) : "v"(g), "s"(lds_base) : "memory");
}
__device__ __forceinline__ float bf2f(unsigned h) { return __builtin_bit_cast(float, h << 16); }
__device__ __forceinline__ float bflo(unsigned u) { return __builtin_bit_cast(float, u << 16); }
__device__ __forceinline__ float bfhi(unsigned u) { return __builtin_bit_cast(float, u & 0xffff0000u); }
__device__ __forceinline__ unsigned pk_bf16(float lo, float hi) {
  bf16x2 p = {(__bf16)lo, (__bf16)hi};
  return __builtin_bit_cast(unsigned, p);
}
__device__ __forceinline__ float rbf(float f) { return (float)(__bf16)f; }
__device__ __forceinline__ u16 f2bf(float f) { return __builtin_bit_cast(u16, (__bf16)f); }
__device__ __forceinline__ float dot2(unsigned a, unsigned b, float c) {
  return __builtin_amdgcn_fdot2_f32_bf16(__builtin_bit_cast(bf16x2, a), __builtin_bit_cast(bf16x2, b), c, false);
}
__device__ __forceinline__ f32x4 mfma(v4u a, v4u b, f32x4 c) {
  return __builtin_amdgcn_mfma_f32_16x16x32_bf16(__builtin_bit_cast(bf16x8, a), __builtin_bit_cast(bf16x8, b), c, 0, 0, 0);
}
__device__ __forceinline__ v4u tr_frag(LAS unsigned char* lo, LAS unsigned char* hi) {
  const v2u l2 = __builtin_bit_cast(v2u, __builtin_amdgcn_ds_read_tr16_b64_v4i16((LAS s16x4*)lo));
  const v2u h2 = __builtin_bit_cast(v2u, __builtin_amdgcn_ds_read_tr16_b64_v4i16((LAS s16x4*)hi));
  return v4u{l2[0], l2[1], h2[0], h2[1]};
}
template <int CTRL> __device__ __forceinline__ float dppf(float x) {
  return __builtin_bit_cast(float, __builtin_amdgcn_mov_dpp(__builtin_bit_cast(int, x), CTRL, 0xf, 0xf, true));
}
template <int CTRL> __device__ __forceinline__ unsigned dppu(unsigned x) {
  return (unsigned)__builtin_amdgcn_mov_dpp((int)x, CTRL, 0xf, 0xf, true);
}
constexpr int XOR1 = 0xB1, XOR2 = 0x4E, XOR7 = 0x141, XOR8 = 0x128, BCAST = 0x150;
__device__ __forceinline__ float x16_sum(float x) {
  auto s = __builtin_amdgcn_permlane16_swap(__float_as_uint(x), __float_as_uint(x), false, false);
  return __uint_as_float(s[0]) + __uint_as_float(s[1]);
}
__device__ __forceinline__ float xrow_sum(float x) {
  x = x16_sum(x);
  auto t = __builtin_amdgcn_permlane32_swap(__float_as_uint(x), __float_as_uint(x), false, false);
  return __uint_as_float(t[0]) + __uint_as_float(t[1]);
}
__device__ __forceinline__ float xrow_max(float x) {
  auto s = __builtin_amdgcn_permlane16_swap(__float_as_uint(x), __float_as_uint(x), false, false);
  x = fmaxf(__uint_as_float(s[0]), __uint_as_float(s[1]));
  auto t = __builtin_amdgcn_permlane32_swap(__float_as_uint(x), __float_as_uint(x), false, false);
  return fmaxf(__uint_as_float(t[0]), __uint_as_float(t[1]));
}
__device__ __forceinline__ float fold_halves(float x) {
  const float t = dppf<XOR8>(x);
  auto p = __builtin_amdgcn_permlane32_swap(__float_as_uint(t), __float_as_uint(t), false, false);
  return x + __uint_as_float(p[1]);
}
__device__ __forceinline__ float wave_sum(float x) {
  x += dppf<XOR1>(x); x += dppf<XOR2>(x); x += dppf<XOR7>(x); x += dppf<XOR8>(x);
  return xrow_sum(x);
}
__device__ __forceinline__ float wave_max(float x) {
  x = fmaxf(x, dppf<XOR1>(x)); x = fmaxf(x, dppf<XOR2>(x)); x = fmaxf(x, dppf<XOR7>(x)); x = fmaxf(x, dppf<XOR8>(x));
  return xrow_max(x);
}
#define WG_BARRIER() asm volatile("s_waitcnt lgkmcnt(0)\n\ts_barrier" ::: "memory")
#define LDS_WAIT() asm volatile("s_waitcnt lgkmcnt(0)" ::: "memory")
#define SCHED_FENCE() __builtin_amdgcn_sched_barrier(0)
#define RLX_AGENT __ATOMIC_RELAXED, __HIP_MEMORY_SCOPE_AGENT

constexpr float SCL = 0.088388347648318440f * 1.4426950408889634f;
constexpr u64 TAG_MASK = (1ull << 40) - 1;

__device__ __forceinline__ u64 granule(u64 tag, float v) { return (tag << 24) | (u64)((__builtin_bit_cast(unsigned, v) + 0x80u) >> 8); }
#define GVAL(x) __builtin_bit_cast(float, (unsigned)(x) << 8)
__device__ __forceinline__ void gather8(GAS u64* ib, u64 tag, u64 (&gv)[8]) {
#pragma unroll
  for (int i = 0; i < 8; ++i) gv[i] = __hip_atomic_load(ib + 64 * i, RLX_AGENT);
  for (unsigned spin = 0; spin < (1u << 22); ++spin) {
    bool all = true;
#pragma unroll
    for (int i = 0; i < 8; ++i) all = all && (gv[i] >> 24) == tag;
    if (__builtin_amdgcn_ballot_w64(!all) == 0) break;
    __builtin_amdgcn_s_sleep(1);
#pragma unroll
    for (int i = 0; i < 8; ++i)
      if ((gv[i] >> 24) != tag) gv[i] = __hip_atomic_load(ib + 64 * i, RLX_AGENT);
  }
}

typedef float f32x16 __attribute__((ext_vector_type(16)));
constexpr int LAG_FREE = 1 << 20;
#define THROTTLE(lag, i) asm volatile( \
    "s_cmp_ge_i32 %0, 0x100000\n\ts_cbranch_scc1 1f\n\ts_waitcnt vmcnt(%c1)\n\t" \
    "s_cmp_gt_i32 %0, %c5\n\ts_cbranch_scc1 1f\n\ts_waitcnt vmcnt(%c2)\n\t" \
    "s_cmp_gt_i32 %0, %c6\n\ts_cbranch_scc1 1f\n\ts_waitcnt vmcnt(%c3)\n\t" \
    "s_cmp_gt_i32 %0, %c7\n\ts_cbranch_scc1 1f\n\ts_waitcnt vmcnt(%c4)\n1:" \
    :: "s"(lag), "i"(GM_LO + 3), "i"(GM_LO + 2), "i"(GM_LO + 1), "i"(GM_LO), \
       "i"(GM_STEP + DITH(i)), "i"(DITH(i)), "i"(-GM_STEP + DITH(i)) : "memory", "scc")
#define DITH(i) (GM_STEP * ((((i) & 1) * 4 + ((i) & 2) + ((i) >> 2)) * 2 + 1) / 16)

template <int J> __device__ __forceinline__ void throttle(int lag) { THROTTLE(lag, (J & 7)); }

#define QUARTER_OFF(st, d) ((((st) ^ rot) << 14) + ((d) << 18))

struct Sel { bool b8, b4, b2, b1; };
struct Red { float x0, x1, x2, x3; };
template <int CTRL> __device__ __forceinline__ float merge2(float lo, float hi, bool take_hi) {
  const float u = lo + dppf<CTRL>(lo), v = hi + dppf<CTRL>(hi);
  return take_hi ? v : u;
}
template <int J> __device__ __forceinline__ void red_push(Red& r, float d, const Sel& s) {
  if (!(J & 1)) { r.x0 = d; return; }
  d = merge2<XOR8>(r.x0, d, s.b8);
  if (!(J & 2)) { r.x1 = d; return; }
  d = merge2<XOR7>(r.x1, d, s.b4);
  if (!(J & 4)) { r.x2 = d; return; }
  d = merge2<XOR2>(r.x2, d, s.b2);
  if (!(J & 8)) { r.x3 = d; return; }
  r.x0 = merge2<XOR1>(r.x3, d, s.b1);
}
__device__ __forceinline__ float dot8(const v4u k, const v4u q) {
  float d = dot2(k[0], q[0], 0.f);
  d = dot2(k[1], q[1], d); d = dot2(k[2], q[2], d);
  return dot2(k[3], q[3], d);
}
template <int J, int N> struct Unroll {
  template <typename F> static __device__ __forceinline__ void run(F&& f) { f(std::integral_constant<int, J>{}); Unroll<J + 1, N>::run(f); }
};
template <int N> struct Unroll<N, N> { template <typename F> static __device__ __forceinline__ void run(F&&) {} };

__global__ void __launch_bounds__(NTHR, 2) fused_kernel(Args) {
  extern __shared__ __attribute__((aligned(16))) unsigned char smem[];
  LAS unsigned char* L = (LAS unsigned char*)smem;
  const int tid = threadIdx.x, lane = tid & 63, w = __builtin_amdgcn_readfirstlane(tid >> 6);
  v4u kb;
  u64 pc0;
  asm volatile("s_load_dwordx4 %0, %2, 0x0\n\ts_getpc_b64 %1" : "=&s"(kb), "=&s"(pc0) : "s"(__builtin_amdgcn_kernarg_segment_ptr()) : "memory");
  const int xc = blockIdx.x, b = blockIdx.z, h = xc * 4 + blockIdx.y, sx = b * 4 + blockIdx.y;
  const int bid = sx * 8 + xc;
  const int g = lane >> 4, c = lane & 15;

  if (__builtin_expect(w >= NWA, 1)) {
    const int ws = w - NWA;
    const unsigned t_zero = GM_CK ? 0u : (unsigned)__builtin_amdgcn_s_memrealtime();
    const size_t kvoff = (size_t)(b * NH + h) * SK * DH * 2;
    const int voff = lane * 16 + ws * 4096;
    const int rot = sx & GM_ROT;
    asm volatile("s_waitcnt lgkmcnt(0)" : "+s"(kb) :: "memory");
    const unsigned char* kp = (const unsigned char*)(((u64)kb[1] << 32) | kb[0]) + kvoff;
    const unsigned char* vp = (const unsigned char*)(((u64)kb[3] << 32) | kb[2]) + kvoff;
    const rsrc_t rk = __builtin_amdgcn_make_buffer_rsrc((void*)kp, 0, SK * DH * 2, 0x00020000);
    const rsrc_t rv = __builtin_amdgcn_make_buffer_rsrc((void*)vp, 0, SK * DH * 2, 0x00020000);
    v4u ring[16], xr[EXTRA ? EXTRA : 1][16];
#define FINE(i)
#pragma unroll
    for (int e = 0; e <= EXTRA; ++e)
#pragma unroll
      for (int j = 0; j < 16; ++j) {
        if (e > 0 || j > GM_SW0) VMWAIT(GM_SW0);
        if (j % 2 == 0) FINE(e * 8 + j / 2);
        const v4u t_ = ld_kv(rk, voff + (j & 3) * 1024, QUARTER_OFF(e, j >> 2));
        if (e == 0) ring[j] = t_; else xr[e - 1][j] = t_;
        SCHED_FENCE();
      }
    const Sel sel = {(c & 8) != 0, (c & 4) != 0, (c & 2) != 0, (c & 1) != 0};
    WG_BARRIER();
    const v4u q = *(const LAS v4u*)(L + L_Q + 16 * c);
    auto lag_of = [&](int i) -> int {
      if (!GM_CTL) return 0;
      const int e = (int)(unsigned)__builtin_amdgcn_s_memrealtime() - ((GM_P2 * i + (GM_P1 - GM_P2) * (i < GM_I1 ? i : GM_I1)) >> 4);
      if (!GM_CK) return e - (int)t_zero - GM_OFF;
      const int base = *(volatile LAS int*)(L + L_CTL + 16);
      *(volatile LAS int*)(L + L_CTL + 4 * ws) = e;
      const int lg = e - __builtin_amdgcn_readfirstlane(base);
      return __builtin_amdgcn_readfirstlane(i >= GM_GX ? 2 * lg - GM_GC : lg);
    };
    f32x16 zs;
    float mx = -INFINITY;
#pragma unroll
    for (int e = 0; e < EXTRA; ++e) {
      Red ra, rb;
      int lag = 0;
      Unroll<0, 16>::run([&](auto jc) {
        constexpr int j = decltype(jc)::value;
        if (j % 8 == 0) lag = lag_of(2 * (EXTRA + 1 + e) + j / 8);
        float d = dot8(ring[j], q);
        asm volatile("" : "+v"(d));
        throttle<j>(lag);
        ring[j] = ld_kv(rk, voff + (j & 3) * 1024, QUARTER_OFF(EXTRA + 1 + e, j >> 2));
        SCHED_FENCE();
        red_push<j>(ra, d, sel);
        red_push<j>(rb, dot8(xr[e][j], q), sel);
        SCHED_FENCE();
      });
      zs[e == 0 ? 0 : EXTRA + e] = ra.x0; zs[1 + e] = rb.x0;
      mx = fmaxf(mx, fmaxf(ra.x0, rb.x0));
      FINE(30 + EXTRA + 1 + e);
    }
#pragma unroll 1
    for (int n = 2 * EXTRA + 1; n <= NS; ++n) {
      const rsrc_t rs = __builtin_amdgcn_make_buffer_rsrc((void*)(n < NS ? kp : vp), 0, SK * DH * 2, 0x00020000);
      const int st = n & (NS - 1);
      Red ra;
      int lag = 0;
      Unroll<0, 16>::run([&](auto jc) {
        constexpr int j = decltype(jc)::value;
        if (j % 8 == 0) lag = lag_of(2 * n + j / 8);
        float d = dot8(ring[j], q);
        asm volatile("" : "+v"(d));
        throttle<j>(lag);
        ring[j] = ld_kv(rs, voff + (j & 3) * 1024, QUARTER_OFF(st, j >> 2));
        SCHED_FENCE();
        red_push<j>(ra, d, sel);
        SCHED_FENCE();
      });
      zs[n - 1] = ra.x0;
      mx = fmaxf(mx, ra.x0);
      FINE(30 + n);
    }
    const float mc = wave_max(mx) * SCL;
    unsigned mask[4];
#pragma unroll
    for (int r = 0; r < 4; ++r) mask[r] = (c >> 1) == r ? ((c & 1) ? 0xffff0000u : 0x0000ffffu) : 0u;
    float lp = 0.f;
    f32x4 acc = {0.f, 0.f, 0.f, 0.f};
#pragma unroll 1
    for (int n = 1; n <= NS; ++n) {
      if (n <= NS / 2) {
#pragma unroll
        for (int i = 0; i < 2; ++i) {
          const float p = __builtin_amdgcn_exp2f(fmaf(zs[2 * n - 2 + i], SCL, -mc));
          lp += p;
          zs[2 * n - 2 + i] = __uint_as_float(pk_bf16(p, p));
        }
        if (n == NS / 2) {
          const float lsum = wave_sum(lp);
          if (lane == 0) {
            volatile LAS float* ml = (volatile LAS float*)(L + L_ML);
            ml[2 * ws] = mc; ml[2 * ws + 1] = lsum;
            LDS_WAIT();
            (void)__hip_atomic_fetch_add((LAS unsigned*)(L + L_MISC + 4), 1u, __ATOMIC_RELAXED, __HIP_MEMORY_SCOPE_WORKGROUP);
          }
        }
      }
      const float pw = zs[n - 1];
      const unsigned pk = __float_as_uint(pw);
      const int oob = n < NS ? 0 : (1 << 24);
      int lag = 0;
      Unroll<0, 16>::run([&](auto jc) {
        constexpr int j = decltype(jc)::value;
        constexpr int src = ((j & 1) << 3) | ((j & 2) << 1) | ((j & 4) >> 1) | ((j & 8) >> 3);
        if (j % 8 == 0) { lag = lag_of(2 * (NS + n) + j / 8); lag = n < NS ? lag : LAG_FREE; }
        const v4u bf = {dppu<BCAST + src>(pk) & mask[0], dppu<BCAST + src>(pk) & mask[1], dppu<BCAST + src>(pk) & mask[2], dppu<BCAST + src>(pk) & mask[3]};
        acc = mfma(ring[j], bf, acc);
        asm volatile("" : "+v"(acc));
        SCHED_FENCE();
        throttle<j>(lag);
        ring[j] = ld_kv(rv, voff + (j & 3) * 1024, QUARTER_OFF(n & (NS - 1), j >> 2) + oob);
        SCHED_FENCE();
      });
      FINE(30 + NS + n);
    }
    if (c < 8) {
      const unsigned h0 = pk_bf16(acc[0], acc[1]), h1 = pk_bf16(acc[2], acc[3]);
      const unsigned l0 = pk_bf16(acc[0] - bflo(h0), acc[1] - bfhi(h0)), l1 = pk_bf16(acc[2] - bflo(h1), acc[3] - bfhi(h1));
      *(LAS v2u*)(L + L_A + ws * A_STR + 64 * g + 8 * c) = v2u{h0, h1};
      *(LAS v2u*)(L + L_A + (4 + ws) * A_STR + 64 * g + 8 * c) = v2u{l0, l1};
    }
    WG_BARRIER();
    const CAS Args& a = args();
    if (ws == 0 || (ws == 1 && h < 4)) {
      const int cb = h + 32 * ws;
      const float b2v = bf2f(((const GAS u16*)a.b2)[16 * cb + c]);
      const float omg = rbf(1.f - *(const LAS float*)(L + L_MISC));
      const u64 ep1 = *(const LAS u64*)(L + L_MISC + 8);
      const u64 tg = ep1 & TAG_MASK;
      GAS u64* ib = (GAS u64*)(a.ws + W_OX) + (size_t)(b * 36 + cb) * 512 + lane;
      GAS u16* op = (GAS u16*)a.out + b * DM + 16 * cb + c;
      u64 gv[8];
#pragma unroll
      for (int i = 0; i < 8; ++i) gv[i] = ~tg << 24;
      u16 res;
      for (unsigned spin = 0;; ++spin) {
        bool all = true;
        float sum = 0.f;
#pragma unroll
        for (int i = 0; i < 8; ++i) { all = all && (gv[i] >> 24) == tg; sum += GVAL(gv[i]); }
        res = f2bf(xrow_sum(sum) + omg * b2v);
        asm volatile("" : "+v"(res));
        if (__builtin_amdgcn_ballot_w64(!all) == 0 || spin > (1u << 22)) break;
        if (spin == 0) {
          __builtin_amdgcn_s_sleep(GM_GSLEEP);
          if (GM_GT) { const unsigned tp = (unsigned)__builtin_amdgcn_readfirstlane(*(volatile LAS int*)(L + L_CTL + 16)) + ((GM_P2 * 64) >> 4) + GM_GT;
            for (int k = 0; k < 40 && (int)((unsigned)__builtin_amdgcn_s_memrealtime() - tp) < 0; ++k) __builtin_amdgcn_s_sleep(1); }
        } else __builtin_amdgcn_s_sleep(GM_GS2);
#pragma unroll
        for (int i = 0; i < 8; ++i)
          if ((gv[i] >> 24) != tg) gv[i] = __hip_atomic_load(ib + 64 * i, RLX_AGENT);
      }
      if (lane < 16) {
        if (GM_OST == 0) *op = res;
        else if (GM_OST == 1) __hip_atomic_store(op, res, RLX_AGENT);
        else __builtin_nontemporal_store(res, op);
      }
      if (ws == 0 && bid == 0 && lane == 0) __hip_atomic_store((GAS u64*)(a.ws + W_EPOCH), ep1, RLX_AGENT);
    }
    return;
  }

  asm volatile("s_waitcnt lgkmcnt(0)" :: "s"(kb) : "memory");
  __builtin_amdgcn_s_setprio(3);
  const CAS Args& a = args();
  LAS unsigned* done_cnt = (LAS unsigned*)(L + L_MISC + 4);
  u64 pace_t = __builtin_amdgcn_s_memrealtime();
#define PACE(step) do { pace_t += (step); while ((int)(((unsigned)__builtin_amdgcn_s_memrealtime() << 4) - (unsigned)pace_t) < 0) __builtin_amdgcn_s_sleep(2); } while (0)
  if (__builtin_expect(w == 0, 1)) {
    const int ch = b & 1, kq = b >> 1;
    {
      const GAS unsigned char* wqb = (const GAS unsigned char*)a.wq + (size_t)(144 * kq + 8 * (lane >> 5) + ((lane >> 3) & 3)) * (NH * DH * 2)
                                     + h * 256 + ch * 128 + (((((lane & 7) >> 1) ^ g) << 1) | (lane & 1)) * 16;
      unsigned dst = (unsigned)(size_t)(L + L_WO);
#pragma unroll 1
      for (int i = 0; i < 9; ++i) {
        glds16<true>(wqb, dst);
        glds16<true>(wqb + 4 * (NH * DH * 2), dst + 1024);
        wqb += 16 * (NH * DH * 2); dst += 2048;
      }
    }
    asm volatile("" : "+s"(pace_t));
    pace_t = (pace_t + (GM_TA - 40)) << 4;
    u64 e1 = *(const CAS u64*)(a.ws + W_EPOCH);
    const GAS unsigned char* xb = (const GAS unsigned char*)a.x + (c & 7) * (DM * 2) + 288 * kq;
    v4u xa[5];
#pragma unroll
    for (int ks = 0; ks < 4; ++ks) xa[ks] = *(const GAS v4u*)(xb + 64 * ks + 16 * g);
    xa[4] = *(const GAS v4u*)(xb + 256 + 16 * (g & 1));
    asm volatile("s_waitcnt vmcnt(0)" : "+v"(xa[0]), "+v"(xa[1]), "+v"(xa[2]), "+v"(xa[3]), "+v"(xa[4]) :: "memory");
    if (g >= 2) xa[4] = v4u{0u, 0u, 0u, 0u};
    asm volatile("" : "+s"(e1));
    e1 += 1;
    const u64 tag = e1 & TAG_MASK;
    const int q4 = (lane >> 2) & 3, p4 = lane & 3;
    {
      const int sw = 2 * (g & 1) + (q4 >> 1), rb = (4 * (g & 1) + q4) * 128 + 8 * p4;
      GAS u64* qx = (GAS u64*)(a.ws + W_QX);
      GAS u64* qo = qx + ((size_t)(h * NB + 4 * (g & 1)) * 4 + kq) * 128 + 64 * ch + 16 * (g >> 1) + c;
#pragma unroll 1
      for (int pr = 0; pr < 2; ++pr) {
        f32x4 qc[2];
#pragma unroll
        for (int j = 0; j < 2; ++j) {
          qc[j] = f32x4{0.f, 0.f, 0.f, 0.f};
          LAS unsigned char* tb = L + L_WO + rb + (((2 * pr + j) ^ sw) * 32);
#pragma unroll
          for (int ks = 0; ks < 5; ++ks) {
            LAS unsigned char* t2 = tb + ks * 4096 + (ks < 4 ? (g >> 1) * 2048 : 0);
            qc[j] = mfma(xa[ks], tr_frag(t2, t2 + 1024), qc[j]);
          }
        }
#pragma unroll
        for (int r = 0; r < 4; ++r) __hip_atomic_store(qo + r * 512 + 32 * pr, granule(tag, g < 2 ? qc[0][r] : qc[1][r]), RLX_AGENT);
      }
      if (lane == 0) { *(LAS u64*)(L + L_MISC + 8) = e1; *done_cnt = 0u; }
      ((LAS unsigned*)(L + L_A + 8 * A_STR))[lane] = 0u;
      if (lane < 5) ((LAS int*)(L + L_CTL))[lane] = (int)(pace_t >> 4) - GM_TA + GM_OFF;
      if (GM_QSLEEP) __builtin_amdgcn_s_sleep(GM_QSLEEP);
      u64 gv[8];
      gather8(qx + (size_t)(h * NB + b) * 512 + lane, tag, gv);
      ((LAS u16*)(L + L_Q))[lane] = f2bf((GVAL(gv[0]) + GVAL(gv[2])) + (GVAL(gv[4]) + GVAL(gv[6])));
      ((LAS u16*)(L + L_Q))[64 + lane] = f2bf((GVAL(gv[1]) + GVAL(gv[3])) + (GVAL(gv[5]) + GVAL(gv[7])));
    }
    WG_BARRIER();
    const GAS unsigned char* wob = (const GAS unsigned char*)(a.wo + (size_t)h * DH * DM);
    int so[9];
#pragma unroll
    for (int j = 0; j < 9; ++j) {
      const int sl = 64 * j + lane, rr = sl / 72, ck = sl % 72;
      so[j] = (8 * (rr & 3) + 2 * (rr >> 2)) * 1152 + ((((ck >> 1) ^ ((rr >> 1) & 3)) << 1) | (ck & 1)) * 16;
    }
#pragma unroll 1
    for (int gr = 0; gr < 16; ++gr) {
      const int ro = (32 * (gr >> 2) + 4 * (gr & 1) + ((gr >> 1) & 1)) * 1152;
      GAS u64* pp = (GAS u64*)(a.ws + W_PACE);
      u64 pv = 0;
      const bool rd = GM_CK && gr % GM_CK == GM_CK - 1 && gr > 1;
      if (rd) pv = __hip_atomic_load(pp + ((gr - 1) * 8 + b) * 32 + (lane & 31), RLX_AGENT);
      if (GM_CK && b == 3 && (gr + 1) % GM_CK == GM_CK - 1 && gr > 0) {
        const v4u ev = *(volatile LAS v4u*)(L + L_CTL);
        const int e0 = (int)ev[0] + ((((int)ev[1] - (int)ev[0]) + ((int)ev[2] - (int)ev[0]) + ((int)ev[3] - (int)ev[0])) >> 2);
        if (lane < 8) __hip_atomic_store(pp + (gr * 8 + lane) * 32 + h, (tag << 24) | (u64)(e0 & 0xffffff), RLX_AGENT);
      }
#pragma unroll
      for (int j = 0; j < 9; ++j) {
        PACE(GM_AD * 16 / 144); glds16<GM_WPOL != 0>(wob + ro + so[j], (unsigned)(size_t)(L + L_WO) + gr * 9216 + j * 1024); VMWAIT(GM_AW);
        if (j == 3 && rd) {
          const int old = *(volatile LAS int*)(L + L_CTL + 16);
          const float df = (float)((((int)(unsigned)pv - old) << 8) >> 8);
          const int adj = (int)(wave_sum(df) * (1.f / 64.f));
          if (__builtin_amdgcn_ballot_w64((pv >> 24) != tag) == 0 && lane == 0) *(volatile LAS int*)(L + L_CTL + 16) = old + adj - GM_L0;
        }
      }
    }
    VMWAIT(0);
  } else {
    const int m = w - 1;
    pace_t = (pace_t + (GM_TA - 40)) << 4;
    {
      const rsrc_t rc = __builtin_amdgcn_make_buffer_rsrc(m == 0 ? (void*)(pc0 + sx * GM_CODE) : (void*)(a.v + (size_t)(b * NH + h) * SK * DH), 0,
                                                          m == 0 ? GM_CODE : m == 1 && GM_VT ? 4 : 0, 0x00020000);
      const unsigned t0 = __builtin_amdgcn_raw_buffer_load_b32(rc, lane * 128, 0, 0);
      const CAS Touch& t = a.touch[m];
      const rsrc_t rp = __builtin_amdgcn_make_buffer_rsrc((void*)(t.base + (xc * t.mx + sx * t.ms)), 0, t.span, 0x00020000);
      unsigned po[3], pf[3];
#pragma unroll
      for (int i = 0; i < 3; ++i) { const unsigned id = 64 * i + lane; po[i] = id * 128 + ((id * 57) >> 9) * t.sb; }
      asm volatile("" : "+v"(po[0]), "+v"(po[1]), "+v"(po[2]) : "v"(t0));
      if (GM_TT) { while ((int)(((unsigned)__builtin_amdgcn_s_memrealtime() << 4) - ((unsigned)pace_t - (GM_TA - GM_TT) * 16)) < 0) __builtin_amdgcn_s_sleep(2); }
      else WG_BARRIER();
#pragma unroll
      for (int i = 0; i < 3; ++i) {
        if (GM_TP) { while ((int)(((unsigned)__builtin_amdgcn_s_memrealtime() << 4) - ((unsigned)pace_t - (GM_TA - GM_TS - (3 * i + m) * GM_TP) * 16)) < 0) __builtin_amdgcn_s_sleep(2); }
        pf[i] = __builtin_amdgcn_raw_buffer_load_b32(rp, po[i], 0, GM_TAUX); if (GM_TW) VMWAIT(0); SCHED_FENCE(); }
      SCHED_FENCE();
      if (GM_TT) WG_BARRIER();
      asm volatile("" :: "v"(pf[0]), "v"(pf[1]), "v"(pf[2]));
    }
    const GAS u16* xp = (const GAS u16*)(a.x + b * DM);
    float xv[9];
#pragma unroll
    for (int i = 0; i < 9; ++i) xv[i] = bf2f(xp[lane + 64 * i]);
    const int col0 = 64 * ((9 * h) / 4 + m), sl = 4 * (c >> 3) + g;
    constexpr int W1S = GM_W1C ? 8 * FF * 2 : FF * 2;
    const GAS unsigned char* w1p = (const GAS unsigned char*)a.w1 + (size_t)((GM_W1C ? 1 : 72) * sl) * (FF * 2) + col0 * 2 + (c & 7) * 16;
    constexpr int R1 = GM_R1;
    v4u r1[R1];
#pragma unroll
    for (int i = 0; i < R1; ++i) { r1[i] = ld_nt((const GAS v4u*)(w1p + i * W1S)); SCHED_FENCE(); }
    float b1v[4];
#pragma unroll
    for (int r = 0; r < 4; ++r) b1v[r] = bf2f(((const GAS u16*)a.b1)[col0 + 8 * (4 * (g & 1) + r) + (c & 7)]);
    if (m == 0) {
      const GAS u16* gp = (const GAS u16*)a.gw;
      float gs = 0.f;
#pragma unroll
      for (int i = 0; i < 9; ++i) gs += rbf(xv[i] * bf2f(gp[lane + 64 * i]));
      const float logit = rbf(wave_sum(gs));
      const float gate = rbf(1.f / (1.f + __expf(-logit)));
      if (lane == 0) *(LAS float*)(L + L_MISC) = gate;
    }
    float sum = 0.f;
#pragma unroll
    for (int i = 0; i < 9; ++i) sum += xv[i];
    const float mean = wave_sum(sum) * (1.f / DM);
    float sq = 0.f;
#pragma unroll
    for (int i = 0; i < 9; ++i) sq += (xv[i] - mean) * (xv[i] - mean);
    const float var = rbf(wave_sum(sq) * (1.f / DM));
    const float mb = rbf(mean), sd = rbf(sqrtf(rbf(var + 1e-5f)));
    LAS u16* yl = (LAS u16*)(L + L_Y + m * 1152);
#pragma unroll
    for (int i = 0; i < 9; ++i) yl[GM_W1C ? 72 * (lane & 7) + (lane >> 3) + 8 * i : lane + 64 * i] = f2bf(rbf(xv[i] - mb) / sd);
    LDS_WAIT();
    unsigned mask[4];
#pragma unroll
    for (int r = 0; r < 4; ++r) mask[r] = ((c & 7) >> 1) == r ? ((c & 1) ? 0xffff0000u : 0x0000ffffu) : 0u;
    f32x4 h1 = {0.f, 0.f, 0.f, 0.f};
    static_assert(24 % R1 == 0, "ring");
#pragma unroll 1
    for (int o = 0; o < 72; o += 24) {
      v4u yy[3];
#pragma unroll
      for (int i = 0; i < 3; ++i) yy[i] = *(const LAS v4u*)(yl + 72 * sl + o + 8 * i);
#pragma unroll
      for (int i = 0; i < 24; ++i) {
        const unsigned u = yy[i >> 3][(i >> 1) & 3];
        const unsigned dup = __builtin_amdgcn_perm(u, u, (i & 1) ? 0x03020302u : 0x01000100u);
        h1 = mfma(r1[i % R1], v4u{dup & mask[0], dup & mask[1], dup & mask[2], dup & mask[3]}, h1);
        SCHED_FENCE();
        const int nx = o + i + R1 < 72 ? o + i + R1 : 71;
        PACE(GM_AD * 16 / 144);
        r1[i % R1] = ld_nt((const GAS v4u*)(w1p + (size_t)nx * W1S));
        SCHED_FENCE();
      }
    }
    const int lo = col0 > FS * h ? col0 : FS * h, hi = col0 + 64 < FS * h + FS ? col0 + 64 : FS * h + FS;
    const int nrg = (hi - lo) >> 3;
    const unsigned step2 = (GM_AD * 16 / 18) / nrg;
    const GAS unsigned char* w2p = (const GAS unsigned char*)a.w2 + (size_t)(lo + sl) * (DM * 2) + (c & 7) * 16;
    v4u r2[3];
#pragma unroll
    for (int i = 0; i < 3; ++i) { r2[i] = ld_nt((const GAS v4u*)(w2p + i * 128)); SCHED_FENCE(); }
    LAS u16* hid = (LAS u16*)(L + L_HID + m * 128);
#pragma unroll
    for (int r = 0; r < 4; ++r) {
      const float pre = rbf(rbf(fold_halves(h1[r])) + b1v[r]);
      const float inner = 0.7978845608028654f * (pre + 0.044715f * pre * pre * pre);
      const float th = 1.f - 2.f / (1.f + __expf(2.f * inner));
      if (lane < 32 && c < 8) hid[8 * (4 * g + r) + c] = f2bf(0.5f * pre * (1.f + th));
    }
    LDS_WAIT();
    f32x4 o2[9];
#pragma unroll
    for (int j = 0; j < 9; ++j) o2[j] = f32x4{0.f, 0.f, 0.f, 0.f};
#pragma unroll 1
    for (int rg = 0; rg < nrg; ++rg) {
      const unsigned u = hid[lo - col0 + 8 * rg + sl];
      const unsigned dup = u | (u << 16);
      const v4u bf = {dup & mask[0], dup & mask[1], dup & mask[2], dup & mask[3]};
      const GAS unsigned char* cur = w2p + (size_t)rg * (8 * DM * 2);
      const GAS unsigned char* nxt = w2p + (size_t)(rg + 1 < nrg ? rg + 1 : rg) * (8 * DM * 2);
#pragma unroll
      for (int j = 0; j < 9; ++j) {
        o2[j] = mfma(r2[j % 3], bf, o2[j]);
        SCHED_FENCE();
        PACE(step2);
        r2[j % 3] = ld_nt((const GAS v4u*)(j < 6 ? cur + (j + 3) * 128 : nxt + (j - 6) * 128));
        SCHED_FENCE();
      }
    }
    LAS float* mp = (LAS float*)(L + L_MLP + m * 2304);
#pragma unroll
    for (int j = 0; j < 9; ++j) {
      f32x4 t;
#pragma unroll
      for (int r = 0; r < 4; ++r) t[r] = fold_halves(o2[j][r]);
      if (lane < 32 && c < 8) {
#pragma unroll
        for (int r = 0; r < 4; ++r) mp[64 * j + 8 * (4 * g + r) + c] = t[r];
      }
    }
  }
  if (GM_PT && w == 1) {
    const GAS unsigned char* tp = (const GAS unsigned char*)a.ws + W_OX + (size_t)b * 36 * 4096 + (GM_PT == 2 && lane < 32 ? h * 4096 + lane * 128 : (lane & 1) * (36 * 4096 - 128));
    if (lane == 62) tp = (const GAS unsigned char*)(a.out + b * DM + 16 * h);
    if (lane == 63) tp = (const GAS unsigned char*)(a.b2 + 16 * h);
    const unsigned tv = __hip_atomic_load((GAS unsigned*)tp, RLX_AGENT);
    asm volatile("" :: "v"(tv));
  }
  LDS_WAIT();
  if (lane == 0) (void)__hip_atomic_fetch_add(done_cnt, 1u, __ATOMIC_RELAXED, __HIP_MEMORY_SCOPE_WORKGROUP);
  while (__builtin_amdgcn_readfirstlane(__hip_atomic_load(done_cnt, __ATOMIC_RELAXED, __HIP_MEMORY_SCOPE_WORKGROUP)) < (unsigned)(NWA + NWS)) __builtin_amdgcn_s_sleep(8);
  asm volatile("" ::: "memory");
  v4u bf[9][4];
  {
    const int q4 = (lane >> 2) & 3, p4 = lane & 3;
    LAS unsigned char* tb = L + L_WO + (4 * g + q4) * 1152 + ((w ^ ((2 * g + (q4 >> 1)) & 3)) * 32) + 8 * p4;
#pragma unroll
    for (int j = 0; j < 9; ++j)
#pragma unroll
      for (int ks = 0; ks < 4; ++ks) bf[j][ks] = tr_frag(tb + ks * 32 * 1152 + j * 128, tb + (ks * 32 + 16) * 1152 + j * 128);
  }
  const float gate = *(const LAS float*)(L + L_MISC);
  const float omg = rbf(1.f - gate);
  const u64 tag = *(const LAS u64*)(L + L_MISC + 8) & TAG_MASK;
  const int jg = 2 * (g & 1) + (g >> 1);
  float base[3];
#pragma unroll
  for (int i = 0; i < 3; ++i) {
    const int n = 16 * (w + 4 * (i < 2 ? 4 * i + jg : 8)) + c;
    const LAS float* mp = (const LAS float*)(L + L_MLP);
    base[i] = omg * ((mp[n] + mp[DM + n]) + mp[2 * DM + n]);
  }
  int ox = (((w + 4 * jg) * 32 + h) * 16 + c) * 8;
  int ox2 = g == 0 ? ox + 2 * 16 * 4096 : 36 * 4096;
  f32x4 cw;
  {
    const f32x4 ml0 = *(const LAS f32x4*)(L + L_ML), ml1 = *(const LAS f32x4*)(L + L_ML + 16);
    const float mx = fmaxf(fmaxf(ml0[0], ml0[2]), fmaxf(ml1[0], ml1[2]));
    cw = f32x4{__builtin_amdgcn_exp2f(ml0[0] - mx), __builtin_amdgcn_exp2f(ml0[2] - mx), __builtin_amdgcn_exp2f(ml1[0] - mx), __builtin_amdgcn_exp2f(ml1[2] - mx)};
    const float den = (cw[0] * ml0[1] + cw[1] * ml0[3]) + (cw[2] * ml1[1] + cw[3] * ml1[3]);
    cw *= gate / den;
  }
#pragma unroll
  for (int j = 0; j < 9; ++j) asm volatile("" : "+v"(bf[j][0]), "+v"(bf[j][1]), "+v"(bf[j][2]), "+v"(bf[j][3]));
  asm volatile("" : "+v"(base[0]), "+v"(base[1]), "+v"(base[2]), "+v"(ox), "+v"(ox2), "+v"(cw));
#pragma unroll 1
  for (int pass = GM_DRY ? 0 : 1; pass < 2; ++pass) {
    int real = pass;
    asm volatile("" : "+s"(real));
    const rsrc_t ro = __builtin_amdgcn_make_buffer_rsrc((void*)(a.ws + W_OX + (size_t)b * 36 * 4096), 0, real ? 36 * 4096 : 0, 0x00020000);
    if (real) WG_BARRIER();
    v4u af[2][4];
    LAS unsigned char* ap0 = L + L_A + (c < 8 ? c : 8) * A_STR + 16 * g;
    LAS unsigned char* ap1 = L + L_A + (c < 8 ? 8 : c - 8) * A_STR + 16 * g;
#pragma unroll
    for (int v = 0; v < 2; ++v)
#pragma unroll
      for (int ks = 0; ks < 4; ++ks) af[v][ks] = *(const LAS v4u*)((v ? ap1 : ap0) + 64 * ks);
    SCHED_FENCE();
    f32x4 oc[5];
#pragma unroll
    for (int p = 0; p < 5; ++p) oc[p] = f32x4{0.f, 0.f, 0.f, 0.f};
    auto put = [&](int i, f32x4 u, f32x4 v) {
      const float tu = fmaf(cw[3], u[3], fmaf(cw[2], u[2], fmaf(cw[1], u[1], cw[0] * u[0])));
      const float tv = fmaf(cw[3], v[3], fmaf(cw[2], v[2], fmaf(cw[1], v[1], cw[0] * v[0])));
      auto sw = __builtin_amdgcn_permlane16_swap(__float_as_uint(tu), __float_as_uint(tv), false, false);
      const u64 gr = granule(tag, (__uint_as_float(sw[0]) + __uint_as_float(sw[1])) + base[i]);
      __builtin_amdgcn_raw_buffer_store_b64(__builtin_bit_cast(v2u, gr), ro, i < 2 ? ox : ox2, i == 1 ? 16 * 4096 : 0, 16);
    };
#pragma unroll
    for (int ks = 0; ks < 4; ++ks) {
      oc[0] = mfma(af[0][ks], bf[0][ks], oc[0]); oc[1] = mfma(af[0][ks], bf[2][ks], oc[1]); oc[4] = mfma(af[0][ks], bf[8][ks], oc[4]);
      SCHED_FENCE();
    }
#pragma unroll
    for (int ks = 0; ks < 4; ++ks) { oc[0] = mfma(af[1][ks], bf[1][ks], oc[0]); oc[1] = mfma(af[1][ks], bf[3][ks], oc[1]); }
    put(2, oc[4], oc[4]);
    SCHED_FENCE();
    oc[2] = mfma(af[0][0], bf[4][0], oc[2]); oc[3] = mfma(af[0][0], bf[6][0], oc[3]);
    SCHED_FENCE();
#pragma unroll
    for (int s8 = 1; s8 < 8; ++s8) { oc[2] = mfma(af[s8 >> 2][s8 & 3], bf[4 + (s8 >> 2)][s8 & 3], oc[2]); oc[3] = mfma(af[s8 >> 2][s8 & 3], bf[6 + (s8 >> 2)][s8 & 3], oc[3]); }
    put(0, oc[0], oc[1]);
#pragma unroll
    for (int i = 0; i < 14; ++i) { __builtin_amdgcn_sched_group_barrier(0x8, 1, 0); __builtin_amdgcn_sched_group_barrier(0x2, 2, 0); }
    SCHED_FENCE();
    put(1, oc[2], oc[3]);
  }
}

}

extern "C" void kernel_launch(void* const* d_in, const int* in_sizes, int n_in, void* d_out, int out_size, void* d_ws,
                              size_t ws_size, hipStream_t stream) {
  (void)in_sizes; (void)n_in; (void)out_size; (void)ws_size;
  gm::Args a;
  a.x = (const gm::u16*)d_in[0]; a.k = (const gm::u16*)d_in[1]; a.v = (const gm::u16*)d_in[2];
  a.wq = (const gm::u16*)d_in[3]; a.wo = (const gm::u16*)d_in[4];
  a.w1 = (const gm::u16*)d_in[6]; a.b1 = (const gm::u16*)d_in[7]; a.w2 = (const gm::u16*)d_in[8]; a.b2 = (const gm::u16*)d_in[9];
  a.gw = (const gm::u16*)d_in[10];
  a.out = (gm::u16*)d_out; a.ws = (unsigned char*)d_ws;
  a.touch[0] = gm::Touch{(const unsigned char*)a.w1, 1152u, 18u * 9216u, 17u * 9216u + 1152u, 9216u - 1152u};
  a.touch[1] = gm::Touch{(const unsigned char*)a.w2, 576u * 1152u, 18u * 1152u, 18u * 1152u, 0u};
  a.touch[2] = gm::Touch{(const unsigned char*)a.wo, 512u * 1152u, 16u * 1152u, 16u * 1152u, 0u};
  (void)hipFuncSetAttribute((const void*)gm::fused_kernel, hipFuncAttributeMaxDynamicSharedMemorySize, gm::L_BYTES);
  hipLaunchKernelGGL(gm::fused_kernel, dim3(8, 4, 8), dim3(gm::NTHR), gm::L_BYTES, stream, a);
}
```

```cpp
#include <hip/hip_runtime.h>
#include <hip/hip_bf16.h>
#include <stdint.h>
#include <type_traits>

namespace gm {

constexpr int NB = 8, NH = 32, SK = 4096, DH = 128, DM = 576, FF = 4608;
constexpr int FS = FF / NH;
constexpr int NWS = 4, NWA = 4, NTHR = (NWS + NWA) * 64;
constexpr int NS = SK / 64 / NWS;

#define GM_B0 3
#define GM_SW0 5
#define GM_SWW 6
#define GM_AW 2
#define GM_LO 3
#define GM_STEP 50
#define GM_OFF 290
#define GM_P 1722
#define GM_L0 16
#define GM_SB 20
#define GM_CK 4
#define GM_TA 610
#define GM_AD 5700
#define GM_QSLEEP 8
#define GM_GSLEEP 15
#define GM_CODE 640
#define STR2(x) #x
#define STR(x) STR2(x)
#define VMWAIT(n) asm volatile("s_waitcnt vmcnt(" STR(n) ")" ::: "memory")

constexpr int L_WO = 0;
constexpr int A_STR = 272;
constexpr int L_A = 128 * 1152;
constexpr int L_MLP = L_A + 9 * A_STR;
constexpr int L_Y = L_MLP + 3 * 2304;
constexpr int L_HID = L_Y + 3 * 1152;
constexpr int L_ML = L_HID + 3 * 128;
constexpr int L_Q = L_ML + 32;
constexpr int L_BT = L_Q + 256;
constexpr int L_GATE = L_BT + 128, L_DONE = L_GATE + 4, L_EPOCH = L_GATE + 8;
constexpr int L_SEEN = L_GATE + 16;
constexpr int L_SCHED = L_SEEN + 16;
constexpr int L_QRDY = L_SCHED + 8;
constexpr int L_BYTES = L_QRDY + 8;
static_assert(L_BYTES <= 163840 && L_MLP % 16 == 0 && L_ML % 16 == 0 && L_GATE % 16 == 0, "LDS");

constexpr size_t W_EPOCH = 0;
constexpr size_t W_PACE = 4096;
constexpr size_t W_QX = 65536;
constexpr size_t W_OX = W_QX + (size_t)NH * NB * 2048;

#define GAS __attribute__((address_space(1)))
#define LAS __attribute__((address_space(3)))
#define CAS __attribute__((address_space(4)))
typedef unsigned v2u __attribute__((ext_vector_type(2)));
typedef unsigned v4u __attribute__((ext_vector_type(4)));
typedef unsigned v16u __attribute__((ext_vector_type(16)));
typedef float f32x4 __attribute__((ext_vector_type(4)));
typedef float f32x16 __attribute__((ext_vector_type(16)));
typedef short s16x4 __attribute__((ext_vector_type(4)));
typedef __bf16 bf16x2 __attribute__((ext_vector_type(2)));
typedef __bf16 bf16x8 __attribute__((ext_vector_type(8)));
typedef unsigned long long u64;
typedef unsigned short u16;
typedef __amdgpu_buffer_rsrc_t rsrc_t;

struct Args {
  const u16* k; const u16* v; const u16* w1; const u16* w2; const u16* wo; const u16* wq; const u16* x; unsigned char* ws;
  const u16* b1; const u16* b2; const u16* gw; u16* out;
};
__device__ __forceinline__ const CAS Args& args() {
  const CAS Args* p = (const CAS Args*)__builtin_amdgcn_kernarg_segment_ptr();
  asm volatile("" : "+s"(p));
  return *p;
}
constexpr int AUX_NT = 2, AUX_SC1 = 16;
__device__ __forceinline__ rsrc_t mk_rsrc(const void* p, int bytes) { return __builtin_amdgcn_make_buffer_rsrc((void*)p, 0, bytes, 0x00020000); }
__device__ __forceinline__ v4u ld_kv(rsrc_t rs, int voff, int soff) { return __builtin_amdgcn_raw_buffer_load_b128(rs, voff, soff, AUX_NT); }
template <bool NT> __device__ __forceinline__ void glds16(const GAS unsigned char* g, unsigned lds_base) {
  unsigned keep;
  if (NT) asm volatile("s_mov_b32 %0, m0\n\ts_mov_b32 m0, %2\n\ts_nop 0\n\tglobal_load_lds_dwordx4 %1, off nt\n\ts_mov_b32 m0, %0" : "=&s"(keep) : "v"(g), "s"(lds_base) : "memory");
  else asm volatile("s_mov_b32 %0, m0\n\ts_mov_b32 m0, %2\n\ts_nop 0\n\tglobal_load_lds_dwordx4 %1, off\n\ts_mov_b32 m0, %0" : "=&s"(keep) : "v"(g), "s"(lds_base) : "memory");
}
__device__ __forceinline__ float bf2f(unsigned h) { return __uint_as_float(h << 16); }
__device__ __forceinline__ float bfhi(unsigned u) { return __uint_as_float(u & 0xffff0000u); }
__device__ __forceinline__ unsigned pk_bf16(float lo, float hi) {
  const bf16x2 p = {(__bf16)lo, (__bf16)hi};
  return __builtin_bit_cast(unsigned, p);
}
__device__ __forceinline__ float rbf(float f) { return (float)(__bf16)f; }
__device__ __forceinline__ u16 f2bf(float f) { const __bf16 t = (__bf16)f; return __builtin_bit_cast(u16, t); }
__device__ __forceinline__ float dot2(unsigned a, unsigned b, float c) {
  return __builtin_amdgcn_fdot2_f32_bf16(__builtin_bit_cast(bf16x2, a), __builtin_bit_cast(bf16x2, b), c, false);
}
__device__ __forceinline__ f32x4 mfma(v4u a, v4u b, f32x4 c) {
  return __builtin_amdgcn_mfma_f32_16x16x32_bf16(__builtin_bit_cast(bf16x8, a), __builtin_bit_cast(bf16x8, b), c, 0, 0, 0);
}
__device__ __forceinline__ v4u tr_frag(LAS unsigned char* lo, LAS unsigned char* hi) {
  const v2u l2 = __builtin_bit_cast(v2u, __builtin_amdgcn_ds_read_tr16_b64_v4i16((LAS s16x4*)lo));
  const v2u h2 = __builtin_bit_cast(v2u, __builtin_amdgcn_ds_read_tr16_b64_v4i16((LAS s16x4*)hi));
  return v4u{l2[0], l2[1], h2[0], h2[1]};
}
template <int CTRL> __device__ __forceinline__ float dppf(float x) {
  return __int_as_float(__builtin_amdgcn_mov_dpp(__float_as_int(x), CTRL, 0xf, 0xf, true));
}
template <int CTRL> __device__ __forceinline__ unsigned dppu(unsigned x) {
  return (unsigned)__builtin_amdgcn_mov_dpp((int)x, CTRL, 0xf, 0xf, true);
}
constexpr int XOR1 = 0xB1, XOR2 = 0x4E, XOR7 = 0x141, XOR8 = 0x128, BCAST = 0x150;
__device__ __forceinline__ float x32_sum(float x) {
  auto t = __builtin_amdgcn_permlane32_swap(__float_as_uint(x), __float_as_uint(x), false, false);
  return __uint_as_float(t[0]) + __uint_as_float(t[1]);
}
__device__ __forceinline__ float xrow_sum(float x) {
  auto s = __builtin_amdgcn_permlane16_swap(__float_as_uint(x), __float_as_uint(x), false, false);
  return x32_sum(__uint_as_float(s[0]) + __uint_as_float(s[1]));
}
__device__ __forceinline__ float fold_halves(float x) {
  const float t = dppf<XOR8>(x);
  auto p = __builtin_amdgcn_permlane32_swap(__float_as_uint(t), __float_as_uint(t), false, false);
  return x + __uint_as_float(p[1]);
}
__device__ __forceinline__ float wave_sum(float x) {
  x += dppf<XOR1>(x); x += dppf<XOR2>(x); x += dppf<XOR7>(x); x += dppf<XOR8>(x);
  return xrow_sum(x);
}
__device__ __forceinline__ float wave_max(float x) {
  x = fmaxf(x, dppf<XOR1>(x)); x = fmaxf(x, dppf<XOR2>(x)); x = fmaxf(x, dppf<XOR7>(x)); x = fmaxf(x, dppf<XOR8>(x));
  auto s = __builtin_amdgcn_permlane16_swap(__float_as_uint(x), __float_as_uint(x), false, false);
  x = fmaxf(__uint_as_float(s[0]), __uint_as_float(s[1]));
  auto t = __builtin_amdgcn_permlane32_swap(__float_as_uint(x), __float_as_uint(x), false, false);
  return fmaxf(__uint_as_float(t[0]), __uint_as_float(t[1]));
}
#define WG_BARRIER() asm volatile("s_waitcnt lgkmcnt(0)\n\ts_barrier" ::: "memory")
#define LDS_WAIT() asm volatile("s_waitcnt lgkmcnt(0)" ::: "memory")
#define SCHED_FENCE() __builtin_amdgcn_sched_barrier(0)
#define RLX_AGENT __ATOMIC_RELAXED, __HIP_MEMORY_SCOPE_AGENT
#define RLX_WG __ATOMIC_RELAXED, __HIP_MEMORY_SCOPE_WORKGROUP
__device__ __forceinline__ unsigned now32() { return (unsigned)__builtin_amdgcn_s_memrealtime(); }
__device__ __forceinline__ void lds_signal(LAS unsigned char* p, int lane) {
  LDS_WAIT();
  if (lane == 0) (void)__hip_atomic_fetch_add((LAS unsigned*)p, 1u, RLX_WG);
}
template <int NAP> __device__ __forceinline__ void lds_await(LAS unsigned char* p, unsigned n) {
  while ((unsigned)__builtin_amdgcn_readfirstlane(__hip_atomic_load((LAS unsigned*)p, RLX_WG)) < n) __builtin_amdgcn_s_sleep(NAP);
  asm volatile("" ::: "memory");
}

constexpr float SCL = 0.088388347648318440f * 1.4426950408889634f;
constexpr unsigned SPIN_CAP = 1u << 19;

__device__ __forceinline__ v2u tag_of(u64 e) { return v2u{(unsigned)e & 255u, (unsigned)(e >> 8)}; }
__device__ __forceinline__ v2u granule(v2u tag, float v) { return v2u{((__float_as_uint(v) + 0x80u) & ~255u) | tag[0], tag[1]}; }

constexpr int LAG_FREE = 1 << 20;
#define DITH(i) (GM_STEP * ((((i) & 1) * 4 + ((i) & 2) + ((i) >> 2)) * 2 + 1) / 16)
template <int J> __device__ __forceinline__ void throttle(int lag) {
  asm volatile(
      "s_cmp_ge_i32 %0, 0x100000\n\ts_cbranch_scc1 1f\n\ts_waitcnt vmcnt(%c1)\n\t"
      "s_cmp_gt_i32 %0, %c5\n\ts_cbranch_scc1 1f\n\ts_waitcnt vmcnt(%c2)\n\t"
      "s_cmp_gt_i32 %0, %c6\n\ts_cbranch_scc1 1f\n\ts_waitcnt vmcnt(%c3)\n\t"
      "s_cmp_gt_i32 %0, %c7\n\ts_cbranch_scc1 1f\n\ts_waitcnt vmcnt(%c4)\n1:"
      :: "s"(lag), "i"(GM_LO + 3), "i"(GM_LO + 2), "i"(GM_LO + 1), "i"(GM_LO),
         "i"(GM_STEP + DITH(J & 7)), "i"(DITH(J & 7)), "i"(-GM_STEP + DITH(J & 7)) : "memory", "scc");
}

struct Sel { bool b8, b4, b2, b1; };
struct Red { float x0, x1, x2, x3; };
template <int CTRL> __device__ __forceinline__ float merge2(float lo, float hi, bool take_hi) {
  const float u = lo + dppf<CTRL>(lo), v = hi + dppf<CTRL>(hi);
  return take_hi ? v : u;
}
template <int J> __device__ __forceinline__ void red_push(Red& r, float d, const Sel& s) {
  if (!(J & 1)) { r.x0 = d; return; }
  d = merge2<XOR8>(r.x0, d, s.b8);
  if (!(J & 2)) { r.x1 = d; return; }
  d = merge2<XOR7>(r.x1, d, s.b4);
  if (!(J & 4)) { r.x2 = d; return; }
  d = merge2<XOR2>(r.x2, d, s.b2);
  if (!(J & 8)) { r.x3 = d; return; }
  r.x0 = merge2<XOR1>(r.x3, d, s.b1);
}
template <int J, int N> struct Unroll {
  template <typename F> static __device__ __forceinline__ void run(F&& f) { f(std::integral_constant<int, J>{}); Unroll<J + 1, N>::run(f); }
};
template <int N> struct Unroll<N, N> { template <typename F> static __device__ __forceinline__ void run(F&&) {} };

__global__ void __launch_bounds__(NTHR, 2) fused_kernel(Args) {
  extern __shared__ __attribute__((aligned(16))) unsigned char smem[];
  LAS unsigned char* L = (LAS unsigned char*)smem;
  const int tid = threadIdx.x, lane = tid & 63, w = __builtin_amdgcn_readfirstlane(tid >> 6);
  v16u kb;
  u64 kout, pc0;
  asm volatile("s_load_dwordx16 %0, %3, 0x0\n\ts_load_dwordx2 %1, %3, 0x58\n\ts_getpc_b64 %2" : "=&s"(kb), "=&s"(kout), "=&s"(pc0) : "s"(__builtin_amdgcn_kernarg_segment_ptr()) : "memory");
  const int xc = blockIdx.x, b = blockIdx.z, h = xc * 4 + blockIdx.y, sx = b * 4 + blockIdx.y;
  const int g = lane >> 4, c = lane & 15;

  if (__builtin_expect(w >= NWA, 1)) {
    const int ws = w - NWA;
    const size_t kvoff = (size_t)(b * NH + h) * SK * DH * 2;
    const int voff = lane * 16 + ws * 4096;
    const int rot = sx & 7;
    asm volatile("s_waitcnt lgkmcnt(0)" : "+s"(kb), "+s"(kout) :: "memory");
    auto ptr_of = [&](int i) { return (const unsigned char*)(((u64)kb[2 * i + 1] << 32) | kb[2 * i]); };
    const unsigned char* kp = ptr_of(0) + kvoff;
    const unsigned char* vp = ptr_of(1) + kvoff;
#define LOAD_ST(rs, st, j, extra) ld_kv(rs, voff + ((j) & 3) * 1024, (((st) ^ rot) << 14) + (((j) >> 2) << 18) + (extra))
    v4u ring[16];
    {
      const rsrc_t rk = mk_rsrc(kp, SK * DH * 2);
#pragma unroll
      for (int j = 0; j < 16; ++j) {
        if (j == GM_B0) asm volatile("s_barrier" ::: "memory");
        if (j > GM_SW0) VMWAIT(GM_SW0);
        ring[j] = LOAD_ST(rk, 0, j, 0);
        SCHED_FENCE();
      }
    }
    v4u dump = {0u, 0u, 0u, 0u};
#pragma unroll
    for (int m = 0; m < 3; ++m) {
      const int first = m == 0 ? xc * 1152 + sx * (18 * 9216) : m == 1 ? (xc * 576 + sx * 18) * 1152 : (xc * 512 + sx * 16) * 1152;
      const rsrc_t rp = mk_rsrc(ptr_of(2 + m) + first, m == 0 ? 17 * 9216 + 1152 : m == 1 ? 18 * 1152 : 16 * 1152);
#pragma unroll
      for (int i = 0; i < (m < 2 ? 6 : 5); ++i) {
        const unsigned id = 256 * i + (tid & 255);
        const unsigned off = id * 16 + (m == 0 ? ((id * 3641) >> 18) * (9216 - 1152) : 0);
        VMWAIT(GM_SWW);
        asm volatile("buffer_load_dwordx4 %0, %1, %2, 0 offen" : "+v"(dump) : "v"(off), "s"(rp) : "memory");
        SCHED_FENCE();
      }
    }
    const Sel sel = {(c & 8) != 0, (c & 4) != 0, (c & 2) != 0, (c & 1) != 0};
    unsigned mask[4];
#pragma unroll
    for (int r = 0; r < 4; ++r) mask[r] = (c >> 1) == r ? ((c & 1) ? 0xffff0000u : 0x0000ffffu) : 0u;
    asm volatile("" : "+v"(mask[0]), "+v"(mask[1]), "+v"(mask[2]), "+v"(mask[3]));
    const int cb = h + 32 * ws;
    const unsigned char* wsp = ptr_of(7);
    u16* outp = (u16*)kout + b * DM + 16 * cb;
    lds_await<1>(L + L_QRDY, 1);
    const v4u q = *(const LAS v4u*)(L + L_Q + 16 * c);
    auto lag_of = [&](int i) -> int {
      const int e = (int)now32() - ((GM_P * i) >> 4);
      const v2u bs = *(volatile LAS v2u*)(L + L_SCHED);
      *(volatile LAS int*)(L + L_SEEN + 4 * ws) = e;
      return e - __builtin_amdgcn_readfirstlane(bs[0]) - ((__builtin_amdgcn_readfirstlane(bs[1]) * i) >> 4);
    };
    f32x16 zs;
    float mx = -INFINITY;
#pragma unroll 1
    for (int n = 1; n <= NS; ++n) {
      const rsrc_t rs = mk_rsrc(n < NS ? kp : vp, SK * DH * 2);
      Red ra;
      int lag = 0;
      Unroll<0, 16>::run([&](auto jc) {
        constexpr int j = decltype(jc)::value;
        if (j % 8 == 0) lag = lag_of(2 * n + j / 8);
        float d = dot2(ring[j][3], q[3], dot2(ring[j][2], q[2], dot2(ring[j][1], q[1], dot2(ring[j][0], q[0], 0.f))));
        asm volatile("" : "+v"(d));
        throttle<j>(lag);
        ring[j] = LOAD_ST(rs, n & (NS - 1), j, 0);
        SCHED_FENCE();
        red_push<j>(ra, d, sel);
        SCHED_FENCE();
      });
      zs[n - 1] = ra.x0;
      mx = fmaxf(mx, ra.x0);
    }
    asm volatile("" :: "v"(dump));
    const float mc = wave_max(mx) * SCL;
    const rsrc_t rv = mk_rsrc(vp, SK * DH * 2);
    float lp = 0.f;
    f32x4 acc = {0.f, 0.f, 0.f, 0.f};
#pragma unroll 1
    for (int n = 1; n <= NS; ++n) {
      if (n <= NS / 2) {
#pragma unroll
        for (int i = 0; i < 2; ++i) {
          const float p = __builtin_amdgcn_exp2f(fmaf(zs[2 * n - 2 + i], SCL, -mc));
          lp += p;
          zs[2 * n - 2 + i] = __uint_as_float(pk_bf16(p, p));
        }
        if (n == NS / 2) {
          const float lsum = wave_sum(lp);
          *(volatile LAS v2u*)(L + L_ML + 8 * ws) = v2u{__float_as_uint(mc), __float_as_uint(lsum)};
          lds_signal(L + L_DONE, lane);
        }
      }
      const float pw = zs[n - 1];
      const unsigned pk = __float_as_uint(pw);
      const int oob = n < NS ? 0 : (1 << 24);
      int lag = 0;
      Unroll<0, 16>::run([&](auto jc) {
        constexpr int j = decltype(jc)::value;
        constexpr int src = ((j & 1) << 3) | ((j & 2) << 1) | ((j & 4) >> 1) | ((j & 8) >> 3);
        if (j % 8 == 0) { lag = lag_of(2 * (NS + n) + j / 8); lag = n < NS ? lag : LAG_FREE; }
        const v4u bf = {dppu<BCAST + src>(pk) & mask[0], dppu<BCAST + src>(pk) & mask[1], dppu<BCAST + src>(pk) & mask[2], dppu<BCAST + src>(pk) & mask[3]};
        acc = mfma(ring[j], bf, acc);
        asm volatile("" : "+v"(acc));
        SCHED_FENCE();
        throttle<j>(lag);
        ring[j] = LOAD_ST(rv, n & (NS - 1), j, oob);
        SCHED_FENCE();
      });
    }
    if (c < 8) {
      const unsigned h0 = pk_bf16(acc[0], acc[1]), h1 = pk_bf16(acc[2], acc[3]);
      const unsigned l0 = pk_bf16(acc[0] - bf2f(h0), acc[1] - bfhi(h0)), l1 = pk_bf16(acc[2] - bf2f(h1), acc[3] - bfhi(h1));
      *(LAS v2u*)(L + L_A + ws * A_STR + 64 * g + 8 * c) = v2u{h0, h1};
      *(LAS v2u*)(L + L_A + (4 + ws) * A_STR + 64 * g + 8 * c) = v2u{l0, l1};
    }
    WG_BARRIER();
    if (ws == 0 || (ws == 1 && h < 4)) {
      const rsrc_t ri = mk_rsrc(wsp + W_OX + (size_t)(b * 36 + cb) * 4096, 4096);
      const float bt = ((const LAS float*)(L + L_BT))[16 * ws + c];
      const u64 ep1 = *(const LAS u64*)(L + L_EPOCH);
      const v2u tg = tag_of(ep1);
      v2u gv[8];
#pragma unroll
      for (int i = 0; i < 8; ++i) gv[i] = ~tg;
      unsigned res, spin = 0;
      asm volatile("" : "+s"(spin), "+v"(gv[0]), "+v"(gv[1]), "+v"(gv[2]), "+v"(gv[3]), "+v"(gv[4]), "+v"(gv[5]), "+v"(gv[6]), "+v"(gv[7]));
      for (;; ++spin) {
        unsigned lo = 0u, hi = 0u;
        float v[8];
#pragma unroll
        for (int i = 0; i < 8; ++i) { lo |= gv[i][0] ^ tg[0]; hi |= gv[i][1] ^ tg[1]; v[i] = __uint_as_float(gv[i][0] & ~255u); }
        const float sum = xrow_sum(((v[0] + v[1]) + (v[2] + v[3])) + ((v[4] + v[5]) + (v[6] + v[7]))) + bt;
        res = pk_bf16(sum, sum);
        asm volatile("" : "+v"(res));
        if (__builtin_amdgcn_ballot_w64(((lo & 255u) | hi) != 0u) == 0 || spin > SPIN_CAP) break;
        if (spin == 0) __builtin_amdgcn_s_sleep(GM_GSLEEP); else __builtin_amdgcn_s_sleep(1);
#pragma unroll
        for (int i = 0; i < 8; ++i) gv[i] = __builtin_amdgcn_raw_buffer_load_b64(ri, lane * 8 + 512 * i, 0, AUX_SC1);
        asm volatile("" ::: "memory");
      }
      if (lane < 16) ((GAS u16*)outp)[c] = (u16)res;
      if (ws == 0 && (sx | xc) == 0 && lane == 0) __hip_atomic_store((GAS u64*)(wsp + W_EPOCH), ep1, RLX_AGENT);
    }
    return;
  }

  asm volatile("s_waitcnt lgkmcnt(0)" :: "s"(kb), "s"(kout) : "memory");
  __builtin_amdgcn_s_setprio(3);
  const CAS Args& a = args();
  unsigned t0 = now32();
  const int q4 = (lane >> 2) & 3, p4 = lane & 3;
  unsigned pt;
#define PACE(step) do { pt += (step); while ((int)((now32() << 4) - pt) < 0) __builtin_amdgcn_s_sleep(2); } while (0)
  if (__builtin_expect(w == 0, 1)) {
    const int ch = b & 1, kq = b >> 1;
    {
      const GAS unsigned char* wqb = (const GAS unsigned char*)a.wq + (size_t)(144 * kq + 8 * (lane >> 5) + ((lane >> 3) & 3)) * (NH * DH * 2)
                                     + h * 256 + ch * 128 + (((((lane & 7) >> 1) ^ g) << 1) | (lane & 1)) * 16;
      unsigned dst = (unsigned)(size_t)(L + L_WO);
#pragma unroll 1
      for (int i = 0; i < 9; ++i) {
        glds16<true>(wqb, dst);
        glds16<true>(wqb + 4 * (NH * DH * 2), dst + 1024);
        wqb += 16 * (NH * DH * 2); dst += 2048;
      }
    }
    u64 e1 = *(const CAS u64*)(a.ws + W_EPOCH);
    const GAS unsigned char* xb = (const GAS unsigned char*)a.x + (c & 7) * (DM * 2) + 288 * kq;
    v4u xa[5];
#pragma unroll
    for (int ks = 0; ks < 4; ++ks) xa[ks] = *(const GAS v4u*)(xb + 64 * ks + 16 * g);
    xa[4] = *(const GAS v4u*)(xb + 256 + 16 * (g & 1));
    SCHED_FENCE();
    asm volatile("s_barrier" ::: "memory");
    asm volatile("" : "+s"(e1), "+s"(t0));
    e1 += 1;
    if (lane == 0) { *(LAS u64*)(L + L_EPOCH) = e1; *(LAS unsigned*)(L + L_DONE) = 0u; *(LAS v2u*)(L + L_SCHED) = v2u{t0 + GM_OFF, 0u}; }
    if (lane < 4) ((LAS unsigned*)(L + L_SEEN))[lane] = t0 + GM_OFF;
    ((LAS unsigned*)(L + L_A + 8 * A_STR))[lane] = 0u;
    {
      v4u tg4;
#pragma unroll
      for (int k = 0; k < 4; ++k) tg4[k] = (unsigned)(k ? (e1 >> (10 * k)) ^ e1 : e1) & 1023u;
      const int sw = 2 * (g & 1) + (q4 >> 1), rb = (4 * (g & 1) + q4) * 128 + 8 * p4;
      const int qoff = (((c & 7) * 4 + kq) * 32 + ch * 16 + 4 * (c >> 3) + g) * 16;
      const rsrc_t rq = mk_rsrc(a.ws + W_QX + (size_t)h * (NB * 2048), NB * 2048);
      asm volatile("s_waitcnt vmcnt(0)" : "+v"(xa[0]), "+v"(xa[1]), "+v"(xa[2]), "+v"(xa[3]), "+v"(xa[4]) :: "memory");
      if (g >= 2) xa[4] = v4u{0u, 0u, 0u, 0u};
#pragma unroll 1
      for (int pr = 0; pr < 2; ++pr) {
        f32x4 qc[2];
#pragma unroll
        for (int j = 0; j < 2; ++j) {
          qc[j] = f32x4{0.f, 0.f, 0.f, 0.f};
          LAS unsigned char* tb = L + L_WO + rb + (((2 * pr + j) ^ sw) * 32);
#pragma unroll
          for (int ks = 0; ks < 5; ++ks) {
            LAS unsigned char* t2 = tb + ks * 4096 + (ks < 4 ? (g >> 1) * 2048 : 0);
            qc[j] = mfma(tr_frag(t2, t2 + 1024), xa[ks], qc[j]);
          }
        }
        v4u gr;
#pragma unroll
        for (int r = 0; r < 4; ++r) gr[r] = ((__float_as_uint(c < 8 ? qc[0][r] : qc[1][r]) + 0x200u) & ~1023u) | tg4[r];
        __builtin_amdgcn_raw_buffer_store_b128(gr, rq, qoff + 128 * pr, 0, AUX_SC1);
      }
      v4u g0 = ~tg4, g1 = ~tg4;
      unsigned spin = 0;
      asm volatile("" : "+v"(g0), "+v"(g1), "+s"(spin));
      for (;; ++spin) {
        const v4u d = ((g0 ^ tg4) | (g1 ^ tg4)) & 1023u;
        float qv[4];
#pragma unroll
        for (int r = 0; r < 4; ++r) qv[r] = x32_sum(__uint_as_float(g0[r] & ~1023u) + __uint_as_float(g1[r] & ~1023u));
        *(LAS v2u*)(L + L_Q + 8 * (lane & 31)) = v2u{pk_bf16(qv[0], qv[1]), pk_bf16(qv[2], qv[3])};
        if (__builtin_amdgcn_ballot_w64(((d[0] | d[1]) | (d[2] | d[3])) != 0u) == 0 || spin > SPIN_CAP) break;
        if (spin == 0) __builtin_amdgcn_s_sleep(GM_QSLEEP); else __builtin_amdgcn_s_sleep(2);
        g0 = __builtin_amdgcn_raw_buffer_load_b128(rq, b * 2048 + lane * 16, 0, AUX_SC1);
        g1 = __builtin_amdgcn_raw_buffer_load_b128(rq, b * 2048 + lane * 16 + 1024, 0, AUX_SC1);
        asm volatile("" ::: "memory");
      }
      lds_signal(L + L_QRDY, lane);
    }
    const GAS unsigned char* wob = (const GAS unsigned char*)(a.wo + (size_t)h * DH * DM);
    int so[9];
#pragma unroll
    for (int j = 0; j < 9; ++j) {
      const int sl = 64 * j + lane, rr = sl / 72, ck = sl % 72;
      so[j] = (8 * (rr & 3) + 2 * (rr >> 2)) * 1152 + ((((ck >> 1) ^ ((rr >> 1) & 3)) << 1) | (ck & 1)) * 16;
    }
    pt = (t0 + GM_TA) << 4;
    const u64 ptag = e1 & ((1ull << 40) - 1);
    int m_prev = (int)t0, s_prev = 0, have = 0;
    float i_prev = 0.f;
#pragma unroll 1
    for (int gr = 0; gr < 16; ++gr) {
      const int ro = (32 * (gr >> 2) + 4 * (gr & 1) + ((gr >> 1) & 1)) * 1152;
      GAS u64* pp = (GAS u64*)(a.ws + W_PACE);
      u64 pv = 0;
      const bool rd = gr % GM_CK == 2 % GM_CK && gr > 1;
      if (rd) asm volatile("global_load_dwordx2 %0, %1, off sc1" : "+v"(pv) : "v"(pp + ((gr - 1) * 8 + b) * 32 + (lane & 31)) : "memory");
      if (b == 3 && (gr + 1) % GM_CK == 2 % GM_CK && gr > 0) {
        const v4u ev = *(volatile LAS v4u*)(L + L_SEEN);
        const int e0 = (int)ev[0] + ((((int)ev[1] - (int)ev[0]) + ((int)ev[2] - (int)ev[0]) + ((int)ev[3] - (int)ev[0])) >> 2);
        if (lane < 8) __hip_atomic_store(pp + (gr * 8 + lane) * 32 + h, (ptag << 24) | (u64)(e0 & 0xffffff), RLX_AGENT);
      }
#pragma unroll
      for (int j = 0; j < 9; ++j) {
        PACE(GM_AD * 16 / 144);
        glds16<false>(wob + ro + so[j], (unsigned)(size_t)(L + L_WO) + gr * 9216 + j * 1024);
        VMWAIT(GM_AW);
        if (j == 3) asm volatile("" : "+v"(pv));
        if (j == 3 && rd) {
          const float df = (float)((((int)(unsigned)pv - m_prev) << 8) >> 8);
          const int mk = m_prev + __builtin_amdgcn_readfirstlane((int)(wave_sum(df) * (1.f / 64.f)));
          if (__builtin_amdgcn_ballot_w64((pv >> 24) != ptag) == 0) {
            const int tk = (int)t0 + GM_TA + (gr - 1) * (GM_AD / 16) - 50;
            const float ik = (float)(tk - mk) * (16.f / GM_P);
            int ds = 0;
            if (have) {
              const int sk = (int)(16.f * (float)(mk - m_prev) / (ik - i_prev)) - GM_SB;
              ds = have > 1 ? (sk < s_prev ? sk : s_prev) : sk / 2;
              ds = ds < 0 ? 0 : ds > 110 ? 110 : ds;
              s_prev = sk;
            }
            if (lane == 0) *(volatile LAS v2u*)(L + L_SCHED) = v2u{(unsigned)(mk - GM_L0 - (int)((float)ds * ik * (1.f / 16.f))), (unsigned)ds};
            m_prev = mk; i_prev = ik; ++have;
          }
        }
      }
    }
    VMWAIT(0);
  } else {
    const int m = w - 1;
    *(volatile LAS unsigned*)(L + L_QRDY) = 0u;
    WG_BARRIER();
    {
      const rsrc_t rc = mk_rsrc(m == 0 ? (const void*)(pc0 + sx * GM_CODE) : (const void*)(a.v + (size_t)(b * NH + h) * SK * DH), m == 0 ? GM_CODE : m == 1 ? 4 : 0);
      const unsigned tc = __builtin_amdgcn_raw_buffer_load_b32(rc, lane * 128, 0, 0);
      asm volatile("" :: "v"(tc));
      lds_await<4>(L + L_QRDY, 1);
    }
    pt = (t0 + GM_TA) << 4;
    const GAS u16* xp = (const GAS u16*)(a.x + b * DM);
    float xv[9];
#pragma unroll
    for (int i = 0; i < 9; ++i) xv[i] = bf2f(xp[lane + 64 * i]);
    const int col0 = 64 * ((9 * h) / 4 + m), sl = 4 * (c >> 3) + g;
    float b1v[4];
#pragma unroll
    for (int r = 0; r < 4; ++r) b1v[r] = bf2f(((const GAS u16*)a.b1)[col0 + 8 * (4 * (g & 1) + r) + (c & 7)]);
    if (m == 0) {
      const GAS u16* gp = (const GAS u16*)a.gw;
      float gs = 0.f;
#pragma unroll
      for (int i = 0; i < 9; ++i) gs += rbf(xv[i] * bf2f(gp[lane + 64 * i]));
      const float logit = rbf(wave_sum(gs));
      const float gate = rbf(1.f / (1.f + __expf(-logit)));
      if (lane == 0) *(LAS float*)(L + L_GATE) = gate;
      if (lane < 16 || (lane < 32 && h < 4)) ((LAS float*)(L + L_BT))[lane] = rbf(1.f - gate) * bf2f(((const GAS u16*)a.b2)[16 * (h + 32 * g) + c]);
    }
    float sum = 0.f;
#pragma unroll
    for (int i = 0; i < 9; ++i) sum += xv[i];
    const float mean = wave_sum(sum) * (1.f / DM);
    float sq = 0.f;
#pragma unroll
    for (int i = 0; i < 9; ++i) sq += (xv[i] - mean) * (xv[i] - mean);
    const float var = rbf(wave_sum(sq) * (1.f / DM));
    const float mb = rbf(mean), sd = rbf(sqrtf(rbf(var + 1e-5f)));
    LAS u16* yl = (LAS u16*)(L + L_Y + m * 1152);
#pragma unroll
    for (int i = 0; i < 9; ++i) yl[72 * (lane & 7) + (lane >> 3) + 8 * i] = f2bf(rbf(xv[i] - mb) / sd);
    LDS_WAIT();
    unsigned mask[4];
#pragma unroll
    for (int r = 0; r < 4; ++r) mask[r] = ((c & 7) >> 1) == r ? ((c & 1) ? 0xffff0000u : 0x0000ffffu) : 0u;
    const int lo = col0 > FS * h ? col0 : FS * h, hi = col0 + 64 < FS * h + FS ? col0 + 64 : FS * h + FS;
    const int nrg = (hi - lo) >> 3;
    const rsrc_t r1s = mk_rsrc(a.w1 + col0, (DM - 1) * FF * 2 + 128);
    const rsrc_t r2s = mk_rsrc(a.w2 + (size_t)lo * DM, nrg * 8 * DM * 2);
    const int v1 = sl * (FF * 2) + (c & 7) * 16, v2 = sl * (DM * 2) + (c & 7) * 16;
    v4u rg3[3];
#pragma unroll
    for (int i = 0; i < 3; ++i) { PACE(GM_AD * 16 / 144); rg3[i] = __builtin_amdgcn_raw_buffer_load_b128(r1s, v1, i * (8 * FF * 2), 0); SCHED_FENCE(); }
    f32x4 h1 = {0.f, 0.f, 0.f, 0.f};
#pragma unroll 1
    for (int o = 0; o < 72; o += 6) {
      const bool last = o == 66;
      const rsrc_t rn = mk_rsrc(last ? a.w2 + (size_t)lo * DM : a.w1 + col0, last ? nrg * 8 * DM * 2 : (DM - 1) * FF * 2 + 128);
      const int vn = last ? v2 : v1;
      unsigned yy[3];
#pragma unroll
      for (int i = 0; i < 3; ++i) yy[i] = *(const LAS unsigned*)(yl + 72 * sl + o + 2 * i);
#pragma unroll
      for (int i = 0; i < 6; ++i) {
        const unsigned dup = __builtin_amdgcn_perm(yy[i >> 1], yy[i >> 1], (i & 1) ? 0x03020302u : 0x01000100u);
        h1 = mfma(rg3[i % 3], v4u{dup & mask[0], dup & mask[1], dup & mask[2], dup & mask[3]}, h1);
        asm volatile("" : "+v"(h1));
        SCHED_FENCE();
        PACE(GM_AD * 16 / 144);
        if (i < 3) rg3[i] = __builtin_amdgcn_raw_buffer_load_b128(r1s, v1, (o + i + 3) * (8 * FF * 2), 0);
        else rg3[i - 3] = __builtin_amdgcn_raw_buffer_load_b128(rn, vn, last ? (i - 3) * 128 : (o + i + 3) * (8 * FF * 2), 0);
        SCHED_FENCE();
      }
    }
    LAS u16* hid = (LAS u16*)(L + L_HID + m * 128);
#pragma unroll
    for (int r = 0; r < 4; ++r) {
      const float pre = rbf(rbf(fold_halves(h1[r])) + b1v[r]);
      const float inner = 0.7978845608028654f * (pre + 0.044715f * pre * pre * pre);
      const float th = 1.f - 2.f / (1.f + __expf(2.f * inner));
      if (lane < 32 && c < 8) hid[8 * (4 * g + r) + c] = f2bf(0.5f * pre * (1.f + th));
    }
    LDS_WAIT();
    const unsigned step2 = (GM_AD * 16 / 18) / nrg;
    f32x4 o2[9];
#pragma unroll
    for (int j = 0; j < 9; ++j) o2[j] = f32x4{0.f, 0.f, 0.f, 0.f};
#pragma unroll 1
    for (int rg = 0; rg < nrg; ++rg) {
      const unsigned u = hid[lo - col0 + 8 * rg + sl];
      const unsigned dup = u | (u << 16);
      const v4u bf = {dup & mask[0], dup & mask[1], dup & mask[2], dup & mask[3]};
#pragma unroll
      for (int j = 0; j < 9; ++j) {
        o2[j] = mfma(rg3[j % 3], bf, o2[j]);
        asm volatile("" : "+v"(o2[j]));
        SCHED_FENCE();
        if (j < 6 || rg + 1 < nrg) PACE(step2);
        rg3[j % 3] = __builtin_amdgcn_raw_buffer_load_b128(r2s, v2 + ((j + 3) % 9) * 128, (rg + (j >= 6)) * (8 * DM * 2), 0);
        SCHED_FENCE();
      }
    }
    LAS float* mp = (LAS float*)(L + L_MLP + m * 2304);
#pragma unroll
    for (int j = 0; j < 9; ++j) {
      f32x4 t;
#pragma unroll
      for (int r = 0; r < 4; ++r) t[r] = fold_halves(o2[j][r]);
      if (lane < 32 && c < 8) {
#pragma unroll
        for (int r = 0; r < 4; ++r) mp[64 * j + 8 * (4 * g + r) + c] = t[r];
      }
    }
  }
  lds_signal(L + L_DONE, lane);
  lds_await<8>(L + L_DONE, NWA + NWS);
  v4u bf[9][4];
  {
    LAS unsigned char* tb = L + L_WO + (4 * g + q4) * 1152 + ((w ^ ((2 * g + (q4 >> 1)) & 3)) * 32) + 8 * p4;
#pragma unroll
    for (int j = 0; j < 9; ++j)
#pragma unroll
      for (int ks = 0; ks < 4; ++ks) bf[j][ks] = tr_frag(tb + ks * 32 * 1152 + j * 128, tb + (ks * 32 + 16) * 1152 + j * 128);
  }
  const float gate = *(const LAS float*)(L + L_GATE);
  const float omg = rbf(1.f - gate);
  v2u tag = tag_of(*(const LAS u64*)(L + L_EPOCH));
  const int jg = 2 * (g & 1) + (g >> 1);
  float base[3];
#pragma unroll
  for (int i = 0; i < 3; ++i) {
    const int n = 16 * (w + 4 * (i < 2 ? 4 * i + jg : 8)) + c;
    const LAS float* mp = (const LAS float*)(L + L_MLP);
    base[i] = omg * ((mp[n] + mp[DM + n]) + mp[2 * DM + n]);
  }
  int ox = (((w + 4 * jg) * 32 + h) * 16 + c) * 8;
  int ox2 = g == 0 ? ox + 2 * 16 * 4096 : 36 * 4096;
  f32x4 cw;
  {
    const f32x4 ml0 = *(const LAS f32x4*)(L + L_ML), ml1 = *(const LAS f32x4*)(L + L_ML + 16);
    const float mx = fmaxf(fmaxf(ml0[0], ml0[2]), fmaxf(ml1[0], ml1[2]));
    cw = f32x4{__builtin_amdgcn_exp2f(ml0[0] - mx), __builtin_amdgcn_exp2f(ml0[2] - mx), __builtin_amdgcn_exp2f(ml1[0] - mx), __builtin_amdgcn_exp2f(ml1[2] - mx)};
    const float den = (cw[0] * ml0[1] + cw[1] * ml0[3]) + (cw[2] * ml1[1] + cw[3] * ml1[3]);
    cw *= gate / den;
  }
  LAS unsigned char* ap0 = L + L_A + (c < 8 ? c : 8) * A_STR + 16 * g;
  LAS unsigned char* ap1 = L + L_A + (c < 8 ? 8 : c - 8) * A_STR + 16 * g;
  unsigned char* oxb = a.ws + W_OX + (size_t)b * 36 * 4096;
#pragma unroll
  for (int j = 0; j < 9; ++j) asm volatile("" : "+v"(bf[j][0]), "+v"(bf[j][1]), "+v"(bf[j][2]), "+v"(bf[j][3]));
  asm volatile("" : "+v"(base[0]), "+v"(base[1]), "+v"(base[2]), "+v"(ox), "+v"(ox2), "+v"(cw), "+v"(ap0), "+v"(ap1), "+v"(tag), "+s"(oxb));
  const rsrc_t ro = mk_rsrc(oxb, 36 * 4096);
  WG_BARRIER();
  v4u af[2][4];
#pragma unroll
  for (int v = 0; v < 2; ++v)
#pragma unroll
    for (int ks = 0; ks < 4; ++ks) af[v][ks] = *(const LAS v4u*)((v ? ap1 : ap0) + 64 * ks);
  SCHED_FENCE();
  f32x4 oc[5];
#pragma unroll
  for (int p = 0; p < 5; ++p) oc[p] = f32x4{0.f, 0.f, 0.f, 0.f};
  auto put = [&](int i, f32x4 u, f32x4 v) {
    const float tu = fmaf(cw[3], u[3], fmaf(cw[2], u[2], fmaf(cw[1], u[1], cw[0] * u[0])));
    const float tv = fmaf(cw[3], v[3], fmaf(cw[2], v[2], fmaf(cw[1], v[1], cw[0] * v[0])));
    auto sw = __builtin_amdgcn_permlane16_swap(__float_as_uint(tu), __float_as_uint(tv), false, false);
    const v2u gr = granule(tag, (__uint_as_float(sw[0]) + __uint_as_float(sw[1])) + base[i]);
    __builtin_amdgcn_raw_buffer_store_b64(gr, ro, i < 2 ? ox : ox2, i == 1 ? 16 * 4096 : 0, AUX_SC1);
  };
#pragma unroll
  for (int ks = 0; ks < 4; ++ks) {
    oc[0] = mfma(af[0][ks], bf[0][ks], oc[0]); oc[1] = mfma(af[0][ks], bf[2][ks], oc[1]); oc[4] = mfma(af[0][ks], bf[8][ks], oc[4]);
    SCHED_FENCE();
  }
#pragma unroll
  for (int ks = 0; ks < 4; ++ks) { oc[0] = mfma(af[1][ks], bf[1][ks], oc[0]); oc[1] = mfma(af[1][ks], bf[3][ks], oc[1]); }
  put(2, oc[4], oc[4]);
  SCHED_FENCE();
  oc[2] = mfma(af[0][0], bf[4][0], oc[2]); oc[3] = mfma(af[0][0], bf[6][0], oc[3]);
  SCHED_FENCE();
#pragma unroll
  for (int s8 = 1; s8 < 8; ++s8) {
    oc[2] = mfma(af[s8 >> 2][s8 & 3], bf[4 + (s8 >> 2)][s8 & 3], oc[2]);
    oc[3] = mfma(af[s8 >> 2][s8 & 3], bf[6 + (s8 >> 2)][s8 & 3], oc[3]);
  }
  put(0, oc[0], oc[1]);
#pragma unroll
  for (int i = 0; i < 14; ++i) { __builtin_amdgcn_sched_group_barrier(0x8, 1, 0); __builtin_amdgcn_sched_group_barrier(0x2, 2, 0); }
  SCHED_FENCE();
  put(1, oc[2], oc[3]);
}

}

extern "C" void kernel_launch(void* const* d_in, const int* in_sizes, int n_in, void* d_out, int out_size, void* d_ws,
                              size_t ws_size, hipStream_t stream) {
  (void)in_sizes; (void)n_in; (void)out_size; (void)ws_size;
  gm::Args a;
  a.x = (const gm::u16*)d_in[0]; a.k = (const gm::u16*)d_in[1]; a.v = (const gm::u16*)d_in[2];
  a.wq = (const gm::u16*)d_in[3]; a.wo = (const gm::u16*)d_in[4];
  a.w1 = (const gm::u16*)d_in[6]; a.b1 = (const gm::u16*)d_in[7]; a.w2 = (const gm::u16*)d_in[8]; a.b2 = (const gm::u16*)d_in[9];
  a.gw = (const gm::u16*)d_in[10];
  a.out = (gm::u16*)d_out; a.ws = (unsigned char*)d_ws;
  (void)hipFuncSetAttribute((const void*)gm::fused_kernel, hipFuncAttributeMaxDynamicSharedMemorySize, gm::L_BYTES);
  hipLaunchKernelGGL(gm::fused_kernel, dim3(8, 4, 8), dim3(gm::NTHR), gm::L_BYTES, stream, a);
}
```

```cpp
#include <hip/hip_runtime.h>
#include <hip/hip_bf16.h>
#include <stdint.h>
#include <type_traits>

namespace gm {

constexpr int NB = 8, NH = 32, SK = 4096, DH = 128, DM = 576, FF = 4608;
constexpr int FS = FF / NH;
constexpr int NWS = 4, NWA = 4, NTHR = (NWS + NWA) * 64;
constexpr int NS = SK / 64 / NWS;

#define GM_B0 3
#define GM_SW0 5
#define GM_SWW 6
#define GM_AW 2
#define GM_LO 3
#define GM_STEP 50
#define GM_OFF 290
#define GM_P 1722
#define GM_L0 16
#define GM_SB 20
#define GM_CK 4
#define GM_TA 610
#define GM_AD 5700
#define GM_QSLEEP 8
#define GM_GSLEEP 15
#define GM_CODE 640
#define STR2(x) #x
#define STR(x) STR2(x)
#define VMWAIT(n) asm volatile("s_waitcnt vmcnt(" STR(n) ")" ::: "memory")

constexpr int L_WO = 0;
constexpr int A_STR = 272;
constexpr int L_A = 128 * 1152;
constexpr int L_MLP = L_A + 9 * A_STR;
constexpr int L_Y = L_MLP + 3 * 2304;
constexpr int L_HID = L_Y + 3 * 1152;
constexpr int L_LP = L_HID + 3 * 128;
constexpr int L_ML = L_LP + 1024;
constexpr int L_Q = L_ML + 32;
constexpr int L_BT = L_Q + 256;
constexpr int L_GATE = L_BT + 128, L_DONE = L_GATE + 4, L_EPOCH = L_GATE + 8;
constexpr int L_SEEN = L_GATE + 16;
constexpr int L_SCHED = L_SEEN + 16;
constexpr int L_QRDY = L_SCHED + 8;
constexpr int L_BYTES = L_QRDY + 8;
static_assert(L_BYTES <= 163840 && L_MLP % 16 == 0 && L_ML % 16 == 0 && L_GATE % 16 == 0, "LDS");

constexpr size_t W_EPOCH = 0;
constexpr size_t W_PACE = 4096;
constexpr size_t W_QX = 65536;
constexpr size_t W_OX = W_QX + (size_t)NH * NB * 2048;

#define GAS __attribute__((address_space(1)))
#define LAS __attribute__((address_space(3)))
#define CAS __attribute__((address_space(4)))
typedef unsigned v2u __attribute__((ext_vector_type(2)));
typedef unsigned v4u __attribute__((ext_vector_type(4)));
typedef unsigned v16u __attribute__((ext_vector_type(16)));
typedef float f32x4 __attribute__((ext_vector_type(4)));
typedef float f32x16 __attribute__((ext_vector_type(16)));
typedef short s16x4 __attribute__((ext_vector_type(4)));
typedef __bf16 bf16x2 __attribute__((ext_vector_type(2)));
typedef __bf16 bf16x8 __attribute__((ext_vector_type(8)));
typedef unsigned long long u64;
typedef unsigned short u16;
typedef __amdgpu_buffer_rsrc_t rsrc_t;

struct Args {
  const u16* k; const u16* v; const u16* w1; const u16* w2; const u16* wo; const u16* wq; const u16* x; unsigned char* ws;
  const u16* b1; const u16* b2; const u16* gw; u16* out;
};
__device__ __forceinline__ const CAS Args& args() {
  const CAS Args* p = (const CAS Args*)__builtin_amdgcn_kernarg_segment_ptr();
  asm volatile("" : "+s"(p));
  return *p;
}
constexpr int AUX_NT = 2, AUX_SC1 = 16;
__device__ __forceinline__ rsrc_t mk_rsrc(const void* p, int bytes) { return __builtin_amdgcn_make_buffer_rsrc((void*)p, 0, bytes, 0x00020000); }
__device__ __forceinline__ v4u ld_kv(rsrc_t rs, int voff, int soff) { return __builtin_amdgcn_raw_buffer_load_b128(rs, voff, soff, AUX_NT); }
template <bool NT> __device__ __forceinline__ void glds16(const GAS unsigned char* g, unsigned lds_base) {
  unsigned keep;
  if (NT) asm volatile("s_mov_b32 %0, m0\n\ts_mov_b32 m0, %2\n\ts_nop 0\n\tglobal_load_lds_dwordx4 %1, off nt\n\ts_mov_b32 m0, %0" : "=&s"(keep) : "v"(g), "s"(lds_base) : "memory");
  else asm volatile("s_mov_b32 %0, m0\n\ts_mov_b32 m0, %2\n\ts_nop 0\n\tglobal_load_lds_dwordx4 %1, off\n\ts_mov_b32 m0, %0" : "=&s"(keep) : "v"(g), "s"(lds_base) : "memory");
}
__device__ __forceinline__ float bf2f(unsigned h) { return __uint_as_float(h << 16); }
__device__ __forceinline__ float bfhi(unsigned u) { return __uint_as_float(u & 0xffff0000u); }
__device__ __forceinline__ unsigned pk_bf16(float lo, float hi) {
  const bf16x2 p = {(__bf16)lo, (__bf16)hi};
  return __builtin_bit_cast(unsigned, p);
}
__device__ __forceinline__ float rbf(float f) { return (float)(__bf16)f; }
__device__ __forceinline__ u16 f2bf(float f) { const __bf16 t = (__bf16)f; return __builtin_bit_cast(u16, t); }
__device__ __forceinline__ float dot2(unsigned a, unsigned b, float c) {
  return __builtin_amdgcn_fdot2_f32_bf16(__builtin_bit_cast(bf16x2, a), __builtin_bit_cast(bf16x2, b), c, false);
}
__device__ __forceinline__ f32x4 mfma(v4u a, v4u b, f32x4 c) {
  return __builtin_amdgcn_mfma_f32_16x16x32_bf16(__builtin_bit_cast(bf16x8, a), __builtin_bit_cast(bf16x8, b), c, 0, 0, 0);
}
__device__ __forceinline__ v4u tr_frag(LAS unsigned char* lo, LAS unsigned char* hi) {
  const v2u l2 = __builtin_bit_cast(v2u, __builtin_amdgcn_ds_read_tr16_b64_v4i16((LAS s16x4*)lo));
  const v2u h2 = __builtin_bit_cast(v2u, __builtin_amdgcn_ds_read_tr16_b64_v4i16((LAS s16x4*)hi));
  return v4u{l2[0], l2[1], h2[0], h2[1]};
}
template <int CTRL> __device__ __forceinline__ float dppf(float x) {
  return __int_as_float(__builtin_amdgcn_mov_dpp(__float_as_int(x), CTRL, 0xf, 0xf, true));
}
template <int CTRL> __device__ __forceinline__ unsigned dppu(unsigned x) {
  return (unsigned)__builtin_amdgcn_mov_dpp((int)x, CTRL, 0xf, 0xf, true);
}
constexpr int XOR1 = 0xB1, XOR2 = 0x4E, XOR7 = 0x141, XOR8 = 0x128, BCAST = 0x150;
__device__ __forceinline__ float x32_sum(float x) {
  auto t = __builtin_amdgcn_permlane32_swap(__float_as_uint(x), __float_as_uint(x), false, false);
  return __uint_as_float(t[0]) + __uint_as_float(t[1]);
}
__device__ __forceinline__ float xrow_sum(float x) {
  auto s = __builtin_amdgcn_permlane16_swap(__float_as_uint(x), __float_as_uint(x), false, false);
  return x32_sum(__uint_as_float(s[0]) + __uint_as_float(s[1]));
}
__device__ __forceinline__ float fold_halves(float x) {
  const float t = dppf<XOR8>(x);
  auto p = __builtin_amdgcn_permlane32_swap(__float_as_uint(t), __float_as_uint(t), false, false);
  return x + __uint_as_float(p[1]);
}
__device__ __forceinline__ float wave_sum(float x) {
  x += dppf<XOR1>(x); x += dppf<XOR2>(x); x += dppf<XOR7>(x); x += dppf<XOR8>(x);
  return xrow_sum(x);
}
__device__ __forceinline__ float wave_max(float x) {
  x = fmaxf(x, dppf<XOR1>(x)); x = fmaxf(x, dppf<XOR2>(x)); x = fmaxf(x, dppf<XOR7>(x)); x = fmaxf(x, dppf<XOR8>(x));
  auto s = __builtin_amdgcn_permlane16_swap(__float_as_uint(x), __float_as_uint(x), false, false);
  x = fmaxf(__uint_as_float(s[0]), __uint_as_float(s[1]));
  auto t = __builtin_amdgcn_permlane32_swap(__float_as_uint(x), __float_as_uint(x), false, false);
  return fmaxf(__uint_as_float(t[0]), __uint_as_float(t[1]));
}
#define WG_BARRIER() asm volatile("s_waitcnt lgkmcnt(0)\n\ts_barrier" ::: "memory")
#define LDS_WAIT() asm volatile("s_waitcnt lgkmcnt(0)" ::: "memory")
#define SCHED_FENCE() __builtin_amdgcn_sched_barrier(0)
#define RLX_AGENT __ATOMIC_RELAXED, __HIP_MEMORY_SCOPE_AGENT
#define RLX_WG __ATOMIC_RELAXED, __HIP_MEMORY_SCOPE_WORKGROUP
__device__ __forceinline__ unsigned now32() { return (unsigned)__builtin_amdgcn_s_memrealtime(); }
__device__ __forceinline__ void lds_signal(LAS unsigned char* p, int lane) {
  LDS_WAIT();
  if (lane == 0) (void)__hip_atomic_fetch_add((LAS unsigned*)p, 1u, RLX_WG);
}
template <int NAP> __device__ __forceinline__ void lds_await(LAS unsigned char* p, unsigned n) {
  while ((unsigned)__builtin_amdgcn_readfirstlane(__hip_atomic_load((LAS unsigned*)p, RLX_WG)) < n) __builtin_amdgcn_s_sleep(NAP);
  asm volatile("" ::: "memory");
}

constexpr float QK_SCALE = 0.088388347648318440f, LOG2E = 1.4426950408889634f;
constexpr unsigned SPIN_CAP = 1u << 21;

__device__ __forceinline__ v2u tag_of(u64 e) { return v2u{(unsigned)e & 255u, (unsigned)(e >> 8)}; }
__device__ __forceinline__ v2u granule(v2u tag, float v) { return v2u{((__float_as_uint(v) + 0x80u) & ~255u) | tag[0], tag[1]}; }

#define DITH(i) (GM_STEP * ((((i) & 1) * 4 + ((i) & 2) + ((i) >> 2)) * 2 + 1) / 16)
template <int J> __device__ __forceinline__ void throttle(int lag) {
  asm volatile(
      "s_waitcnt vmcnt(%c1)\n\t"
      "s_cmp_gt_i32 %0, %c5\n\ts_cbranch_scc1 1f\n\ts_waitcnt vmcnt(%c2)\n\t"
      "s_cmp_gt_i32 %0, %c6\n\ts_cbranch_scc1 1f\n\ts_waitcnt vmcnt(%c3)\n\t"
      "s_cmp_gt_i32 %0, %c7\n\ts_cbranch_scc1 1f\n\ts_waitcnt vmcnt(%c4)\n1:"
      :: "s"(lag), "i"(GM_LO + 3), "i"(GM_LO + 2), "i"(GM_LO + 1), "i"(GM_LO),
         "i"(GM_STEP + DITH(J & 7)), "i"(DITH(J & 7)), "i"(-GM_STEP + DITH(J & 7)) : "memory", "scc");
}

struct Sel { bool b8, b4, b2, b1; };
struct Red { float x0, x1, x2, x3; };
template <int CTRL> __device__ __forceinline__ float merge2(float lo, float hi, bool take_hi) {
  const float u = lo + dppf<CTRL>(lo), v = hi + dppf<CTRL>(hi);
  return take_hi ? v : u;
}
template <int J> __device__ __forceinline__ void red_push(Red& r, float d, const Sel& s) {
  if (!(J & 1)) { r.x0 = d; return; }
  d = merge2<XOR8>(r.x0, d, s.b8);
  if (!(J & 2)) { r.x1 = d; return; }
  d = merge2<XOR7>(r.x1, d, s.b4);
  if (!(J & 4)) { r.x2 = d; return; }
  d = merge2<XOR2>(r.x2, d, s.b2);
  if (!(J & 8)) { r.x3 = d; return; }
  r.x0 = merge2<XOR1>(r.x3, d, s.b1);
}
template <int J, int N> struct Unroll {
  template <typename F> static __device__ __forceinline__ void run(F&& f) { f(std::integral_constant<int, J>{}); Unroll<J + 1, N>::run(f); }
};
template <int N> struct Unroll<N, N> { template <typename F> static __device__ __forceinline__ void run(F&&) {} };

__global__ void __launch_bounds__(NTHR, 2) fused_kernel(Args) {
  extern __shared__ __attribute__((aligned(16))) unsigned char smem[];
  LAS unsigned char* L = (LAS unsigned char*)smem;
  const int tid = threadIdx.x, lane = tid & 63, w = __builtin_amdgcn_readfirstlane(tid >> 6);
  v16u kb;
  u64 kout, pc0;
  asm volatile("s_load_dwordx16 %0, %3, 0x0\n\ts_load_dwordx2 %1, %3, 0x58\n\ts_getpc_b64 %2" : "=&s"(kb), "=&s"(kout), "=&s"(pc0) : "s"(__builtin_amdgcn_kernarg_segment_ptr()) : "memory");
  const int xc = blockIdx.x, b = blockIdx.z, h = xc * 4 + blockIdx.y, sx = b * 4 + blockIdx.y;
  const int g = lane >> 4, c = lane & 15;

  if (__builtin_expect(w >= NWA, 1)) {
    const int ws = w - NWA;
    const size_t kvoff = (size_t)(b * NH + h) * SK * DH * 2;
    const int voff = lane * 16 + ws * 4096;
    const int rot = sx & 7;
    asm volatile("s_waitcnt lgkmcnt(0)" : "+s"(kb), "+s"(kout) :: "memory");
    auto ptr_of = [&](int i) { return (const unsigned char*)(((u64)kb[2 * i + 1] << 32) | kb[2 * i]); };
    const unsigned char* kp = ptr_of(0) + kvoff;
    const unsigned char* vp = ptr_of(1) + kvoff;
#define LOAD_ST(rs, st, j) ld_kv(rs, voff + ((j) & 3) * 1024, (((st) ^ rot) << 14) + (((j) >> 2) << 18))
    v4u ring[16];
    unsigned mask[4];
    LAS unsigned char *qa, *aw;
    {
      const rsrc_t rk = mk_rsrc(kp, SK * DH * 2);
#pragma unroll
      for (int j = 0; j < 16; ++j) {
        if (j == GM_B0) {
          int ln = lane;
          asm volatile("" : "+v"(ln));
          const int cc = ln & 15;
          qa = L + L_Q + 16 * cc;
          aw = L + L_A + ws * A_STR + 64 * (ln >> 4) + 8 * cc;
#pragma unroll
          for (int r = 0; r < 4; ++r) mask[r] = (cc >> 1) == r ? ((cc & 1) ? 0xffff0000u : 0x0000ffffu) : 0u;
          asm volatile("s_barrier" : "+v"(mask[0]), "+v"(mask[1]), "+v"(mask[2]), "+v"(mask[3]), "+v"(qa), "+v"(aw) :: "memory");
        }
        if (j > GM_SW0) VMWAIT(GM_SW0);
        ring[j] = LOAD_ST(rk, 0, j);
        SCHED_FENCE();
      }
    }
    v4u dump = {0u, 0u, 0u, 0u};
#pragma unroll
    for (int m = 0; m < 3; ++m) {
      const int first = m == 0 ? xc * 1152 + sx * (18 * 9216) : m == 1 ? (xc * 576 + sx * 18) * 1152 : (xc * 512 + sx * 16) * 1152;
      const rsrc_t rp = mk_rsrc(ptr_of(2 + m) + first, m == 0 ? 17 * 9216 + 1152 : m == 1 ? 18 * 1152 : 16 * 1152);
#pragma unroll
      for (int i = 0; i < (m < 2 ? 6 : 5); ++i) {
        const unsigned id = 256 * i + (tid & 255);
        const unsigned off = id * 16 + (m == 0 ? ((id * 3641) >> 18) * (9216 - 1152) : 0);
        VMWAIT(GM_SWW);
        asm volatile("buffer_load_dwordx4 %0, %1, %2, 0 offen" : "+v"(dump) : "v"(off), "s"(rp) : "memory");
        SCHED_FENCE();
      }
    }
    const Sel sel = {(c & 8) != 0, (c & 4) != 0, (c & 2) != 0, (c & 1) != 0};
    const int cb = h + 32 * ws;
    const unsigned char* wsp = ptr_of(7);
    u16* outp = (u16*)kout + b * DM + 16 * cb;
    v4u q;
    for (;;) {
      unsigned rdy;
      asm volatile("ds_read_b32 %0, %2\n\tds_read_b128 %1, %3\n\ts_waitcnt lgkmcnt(0)" : "=&v"(rdy), "=&v"(q) : "v"(L + L_QRDY), "v"(qa) : "memory");
      if (__builtin_expect(__builtin_amdgcn_readfirstlane(rdy) != 0, 1)) break;
      __builtin_amdgcn_s_sleep(1);
    }
    auto lag_of = [&](int i) -> int {
      const int e = (int)now32() - ((GM_P * i) >> 4);
      const v2u bs = *(volatile LAS v2u*)(L + L_SCHED);
      *(volatile LAS int*)(L + L_SEEN + 4 * ws) = e;
      return e - __builtin_amdgcn_readfirstlane(bs[0]) - ((__builtin_amdgcn_readfirstlane(bs[1]) * i) >> 4);
    };
    f32x16 zs;
    float mx = -INFINITY;
#pragma unroll 1
    for (int n = 1; n <= NS; ++n) {
      const rsrc_t rs = mk_rsrc(n < NS ? kp : vp, SK * DH * 2);
      Red ra;
      int lag = 0;
      Unroll<0, 16>::run([&](auto jc) {
        constexpr int j = decltype(jc)::value;
        if (j % 8 == 0) lag = lag_of(2 * n + j / 8);
        float d = dot2(ring[j][3], q[3], dot2(ring[j][2], q[2], dot2(ring[j][1], q[1], dot2(ring[j][0], q[0], 0.f))));
        asm volatile("" : "+v"(d));
        throttle<j>(lag);
        ring[j] = LOAD_ST(rs, n & (NS - 1), j);
        SCHED_FENCE();
        red_push<j>(ra, d, sel);
        SCHED_FENCE();
      });
      const float z = rbf(rbf(ra.x0) * QK_SCALE);
      zs[n - 1] = z;
      mx = fmaxf(mx, z);
    }
    asm volatile("" :: "v"(dump));
    const float mc = wave_max(mx);
    const rsrc_t rv = mk_rsrc(vp, SK * DH * 2);
    float lp = 0.f;
    f32x4 acc = {0.f, 0.f, 0.f, 0.f};
#pragma unroll 1
    for (int n = 1; n < NS; ++n) {
      if (n <= NS / 2) {
#pragma unroll
        for (int i = 0; i < 2; ++i) {
          const float p = __builtin_amdgcn_exp2f((zs[2 * n - 2 + i] - mc) * LOG2E);
          lp += p;
          zs[2 * n - 2 + i] = __uint_as_float(pk_bf16(p, p));
        }
        if (n == NS / 2) {
          *(volatile LAS float*)(L + L_LP + 4 * (tid & 255)) = lp;
          *(volatile LAS v2u*)(L + L_ML + 8 * ws) = v2u{__float_as_uint(mc), 1u};
        }
      }
      const float pw = zs[n - 1];
      const unsigned pk = __float_as_uint(pw);
      int lag = 0;
      Unroll<0, 16>::run([&](auto jc) {
        constexpr int j = decltype(jc)::value;
        constexpr int src = ((j & 1) << 3) | ((j & 2) << 1) | ((j & 4) >> 1) | ((j & 8) >> 3);
        if (j % 8 == 0) lag = lag_of(2 * (NS + n) + j / 8);
        const v4u bf = {dppu<BCAST + src>(pk) & mask[0], dppu<BCAST + src>(pk) & mask[1], dppu<BCAST + src>(pk) & mask[2], dppu<BCAST + src>(pk) & mask[3]};
        acc = mfma(ring[j], bf, acc);
        asm volatile("" : "+v"(acc));
        SCHED_FENCE();
        throttle<j>(lag);
        ring[j] = LOAD_ST(rv, n, j);
        SCHED_FENCE();
      });
    }
    {
      const float pw = zs[NS - 1];
      const unsigned pk = __float_as_uint(pw);
      Unroll<0, 16>::run([&](auto jc) {
        constexpr int j = decltype(jc)::value;
        constexpr int src = ((j & 1) << 3) | ((j & 2) << 1) | ((j & 4) >> 1) | ((j & 8) >> 3);
        const v4u bf = {dppu<BCAST + src>(pk) & mask[0], dppu<BCAST + src>(pk) & mask[1], dppu<BCAST + src>(pk) & mask[2], dppu<BCAST + src>(pk) & mask[3]};
        acc = mfma(ring[j], bf, acc);
        SCHED_FENCE();
      });
    }
    if (c < 8) {
      const unsigned h0 = pk_bf16(acc[0], acc[1]), h1 = pk_bf16(acc[2], acc[3]);
      const unsigned l0 = pk_bf16(acc[0] - bf2f(h0), acc[1] - bfhi(h0)), l1 = pk_bf16(acc[2] - bf2f(h1), acc[3] - bfhi(h1));
      *(LAS v2u*)aw = v2u{h0, h1};
      *(LAS v2u*)(aw + 4 * A_STR) = v2u{l0, l1};
    }
    WG_BARRIER();
    if (ws == 0 || (ws == 1 && h < 4)) {
      const rsrc_t ri = mk_rsrc(wsp + W_OX + (size_t)(b * 36 + cb) * 4096, 4096);
      const float bt = ((const LAS float*)(L + L_BT))[16 * ws + c];
      const u64 ep1 = *(const LAS u64*)(L + L_EPOCH);
      const v2u tg = tag_of(ep1);
      v2u gv[8];
#pragma unroll
      for (int i = 0; i < 8; ++i) gv[i] = ~tg;
      unsigned res, spin = 0;
      asm volatile("" : "+s"(spin), "+v"(gv[0]), "+v"(gv[1]), "+v"(gv[2]), "+v"(gv[3]), "+v"(gv[4]), "+v"(gv[5]), "+v"(gv[6]), "+v"(gv[7]));
      for (;; ++spin) {
        unsigned lo = 0u, hi = 0u;
        float v[8];
#pragma unroll
        for (int i = 0; i < 8; ++i) { lo |= gv[i][0] ^ tg[0]; hi |= gv[i][1] ^ tg[1]; v[i] = __uint_as_float(gv[i][0] & ~255u); }
        const float sum = xrow_sum(((v[0] + v[1]) + (v[2] + v[3])) + ((v[4] + v[5]) + (v[6] + v[7]))) + bt;
        res = pk_bf16(sum, sum);
        asm volatile("" : "+v"(res));
        if (__builtin_amdgcn_ballot_w64(((lo & 255u) | hi) != 0u) == 0 || spin > SPIN_CAP) break;
        if (spin == 0) __builtin_amdgcn_s_sleep(GM_GSLEEP); else __builtin_amdgcn_s_sleep(1);
#pragma unroll
        for (int i = 0; i < 8; ++i) gv[i] = __builtin_amdgcn_raw_buffer_load_b64(ri, lane * 8 + 512 * i, 0, AUX_SC1);
        asm volatile("" ::: "memory");
      }
      if (lane < 16) ((GAS u16*)outp)[c] = (u16)res;
      if (ws == 0 && (sx | xc) == 0 && lane == 0) __hip_atomic_store((GAS u64*)(wsp + W_EPOCH), ep1, RLX_AGENT);
    }
    return;
  }

  asm volatile("s_waitcnt lgkmcnt(0)" :: "s"(kb), "s"(kout) : "memory");
  __builtin_amdgcn_s_setprio(3);
  const CAS Args& a = args();
  unsigned t0 = now32();
  const int q4 = (lane >> 2) & 3, p4 = lane & 3;
  unsigned pt;
#define PACE(step) do { pt += (step); while ((int)((now32() << 4) - pt) < 0) __builtin_amdgcn_s_sleep(2); } while (0)
  if (__builtin_expect(w == 0, 1)) {
    const int ch = b & 1, kq = b >> 1;
    {
      const GAS unsigned char* wqb = (const GAS unsigned char*)a.wq + (size_t)(144 * kq + 8 * (lane >> 5) + ((lane >> 3) & 3)) * (NH * DH * 2)
                                     + h * 256 + ch * 128 + (((((lane & 7) >> 1) ^ g) << 1) | (lane & 1)) * 16;
      unsigned dst = (unsigned)(size_t)(L + L_WO);
#pragma unroll 1
      for (int i = 0; i < 9; ++i) {
        glds16<true>(wqb, dst);
        glds16<true>(wqb + 4 * (NH * DH * 2), dst + 1024);
        wqb += 16 * (NH * DH * 2); dst += 2048;
      }
    }
    u64 e1 = *(const CAS u64*)(a.ws + W_EPOCH);
    const GAS unsigned char* xb = (const GAS unsigned char*)a.x + (c & 7) * (DM * 2) + 288 * kq;
    v4u xa[5];
#pragma unroll
    for (int ks = 0; ks < 4; ++ks) xa[ks] = *(const GAS v4u*)(xb + 64 * ks + 16 * g);
    xa[4] = *(const GAS v4u*)(xb + 256 + 16 * (g & 1));
    SCHED_FENCE();
    asm volatile("s_barrier" ::: "memory");
    asm volatile("" : "+s"(e1), "+s"(t0));
    e1 += 1;
    if (lane == 0) { *(LAS u64*)(L + L_EPOCH) = e1; *(LAS unsigned*)(L + L_DONE) = 0u; *(LAS v2u*)(L + L_SCHED) = v2u{t0 + GM_OFF, 0u}; }
    if (lane < 4) ((LAS unsigned*)(L + L_SEEN))[lane] = t0 + GM_OFF;
    if (lane < 8) ((LAS unsigned*)(L + L_ML))[lane] = 0u;
    ((LAS unsigned*)(L + L_A + 8 * A_STR))[lane] = 0u;
    {
      v4u tg4;
#pragma unroll
      for (int k = 0; k < 4; ++k) tg4[k] = (unsigned)(k ? (e1 >> (10 * k)) ^ e1 : e1) & 1023u;
      const int sw = 2 * (g & 1) + (q4 >> 1), rb = (4 * (g & 1) + q4) * 128 + 8 * p4;
      const int qoff = (((c & 7) * 4 + kq) * 32 + ch * 16 + 4 * (c >> 3) + g) * 16;
      const rsrc_t rq = mk_rsrc(a.ws + W_QX + (size_t)h * (NB * 2048), NB * 2048);
      asm volatile("s_waitcnt vmcnt(0)" : "+v"(xa[0]), "+v"(xa[1]), "+v"(xa[2]), "+v"(xa[3]), "+v"(xa[4]) :: "memory");
      if (g >= 2) xa[4] = v4u{0u, 0u, 0u, 0u};
#pragma unroll 1
      for (int pr = 0; pr < 2; ++pr) {
        f32x4 qc[2];
#pragma unroll
        for (int j = 0; j < 2; ++j) {
          qc[j] = f32x4{0.f, 0.f, 0.f, 0.f};
          LAS unsigned char* tb = L + L_WO + rb + (((2 * pr + j) ^ sw) * 32);
#pragma unroll
          for (int ks = 0; ks < 5; ++ks) {
            LAS unsigned char* t2 = tb + ks * 4096 + (ks < 4 ? (g >> 1) * 2048 : 0);
            qc[j] = mfma(tr_frag(t2, t2 + 1024), xa[ks], qc[j]);
          }
        }
        v4u gr;
#pragma unroll
        for (int r = 0; r < 4; ++r) gr[r] = ((__float_as_uint(c < 8 ? qc[0][r] : qc[1][r]) + 0x200u) & ~1023u) | tg4[r];
        __builtin_amdgcn_raw_buffer_store_b128(gr, rq, qoff + 128 * pr, 0, AUX_SC1);
      }
      v4u g0 = ~tg4, g1 = ~tg4;
      unsigned spin = 0;
      asm volatile("" : "+v"(g0), "+v"(g1), "+s"(spin));
      for (;; ++spin) {
        const v4u d = ((g0 ^ tg4) | (g1 ^ tg4)) & 1023u;
        float qv[4];
#pragma unroll
        for (int r = 0; r < 4; ++r) qv[r] = x32_sum(__uint_as_float(g0[r] & ~1023u) + __uint_as_float(g1[r] & ~1023u));
        *(LAS v2u*)(L + L_Q + 8 * (lane & 31)) = v2u{pk_bf16(qv[0], qv[1]), pk_bf16(qv[2], qv[3])};
        if (__builtin_amdgcn_ballot_w64(((d[0] | d[1]) | (d[2] | d[3])) != 0u) == 0 || spin > SPIN_CAP) break;
        if (spin == 0) __builtin_amdgcn_s_sleep(GM_QSLEEP); else __builtin_amdgcn_s_sleep(2);
        g0 = __builtin_amdgcn_raw_buffer_load_b128(rq, b * 2048 + lane * 16, 0, AUX_SC1);
        g1 = __builtin_amdgcn_raw_buffer_load_b128(rq, b * 2048 + lane * 16 + 1024, 0, AUX_SC1);
        asm volatile("" ::: "memory");
      }
      lds_signal(L + L_QRDY, lane);
    }
    const GAS unsigned char* wob = (const GAS unsigned char*)(a.wo + (size_t)h * DH * DM);
    int so[9];
#pragma unroll
    for (int j = 0; j < 9; ++j) {
      const int sl = 64 * j + lane, rr = sl / 72, ck = sl % 72;
      so[j] = (8 * (rr & 3) + 2 * (rr >> 2)) * 1152 + ((((ck >> 1) ^ ((rr >> 1) & 3)) << 1) | (ck & 1)) * 16;
    }
    pt = (t0 + GM_TA) << 4;
    const u64 ptag = e1 & ((1ull << 40) - 1);
    int m_prev = (int)t0, s_prev = 0, have = 0;
    float i_prev = 0.f;
#pragma unroll 1
    for (int gr = 0; gr < 16; ++gr) {
      const int ro = (32 * (gr >> 2) + 4 * (gr & 1) + ((gr >> 1) & 1)) * 1152;
      GAS u64* pp = (GAS u64*)(a.ws + W_PACE);
      u64 pv = 0;
      const bool rd = gr % GM_CK == 2 % GM_CK && gr > 1;
      if (rd) asm volatile("global_load_dwordx2 %0, %1, off sc1" : "+v"(pv) : "v"(pp + ((gr - 1) * 8 + b) * 32 + (lane & 31)) : "memory");
      if (b == 3 && (gr + 1) % GM_CK == 2 % GM_CK && gr > 0) {
        const v4u ev = *(volatile LAS v4u*)(L + L_SEEN);
        const int e0 = (int)ev[0] + ((((int)ev[1] - (int)ev[0]) + ((int)ev[2] - (int)ev[0]) + ((int)ev[3] - (int)ev[0])) >> 2);
        if (lane < 8) __hip_atomic_store(pp + (gr * 8 + lane) * 32 + h, (ptag << 24) | (u64)(e0 & 0xffffff), RLX_AGENT);
      }
#pragma unroll
      for (int j = 0; j < 9; ++j) {
        PACE(GM_AD * 16 / 144);
        glds16<false>(wob + ro + so[j], (unsigned)(size_t)(L + L_WO) + gr * 9216 + j * 1024);
        VMWAIT(GM_AW);
        if (j == 3) asm volatile("" : "+v"(pv));
        if (j == 3 && rd) {
          const float df = (float)((((int)(unsigned)pv - m_prev) << 8) >> 8);
          const int mk = m_prev + __builtin_amdgcn_readfirstlane((int)(wave_sum(df) * (1.f / 64.f)));
          if (__builtin_amdgcn_ballot_w64((pv >> 24) != ptag) == 0) {
            const int tk = (int)t0 + GM_TA + (gr - 1) * (GM_AD / 16) - 50;
            const float ik = (float)(tk - mk) * (16.f / GM_P);
            int ds = 0;
            if (have) {
              const int sk = (int)(16.f * (float)(mk - m_prev) / (ik - i_prev)) - GM_SB;
              ds = have > 1 ? (sk < s_prev ? sk : s_prev) : sk / 2;
              ds = ds < 0 ? 0 : ds > 110 ? 110 : ds;
              s_prev = sk;
            }
            if (lane == 0) *(volatile LAS v2u*)(L + L_SCHED) = v2u{(unsigned)(mk - GM_L0 - (int)((float)ds * ik * (1.f / 16.f))), (unsigned)ds};
            m_prev = mk; i_prev = ik; ++have;
          }
        }
      }
    }
    VMWAIT(0);
  } else {
    const int m = w - 1;
    *(volatile LAS unsigned*)(L + L_QRDY) = 0u;
    WG_BARRIER();
    {
      const rsrc_t rc = mk_rsrc(m == 0 ? (const void*)(pc0 + sx * GM_CODE) : (const void*)(a.v + (size_t)(b * NH + h) * SK * DH), m == 0 ? GM_CODE : m == 1 ? 4 : 0);
      const unsigned tc = __builtin_amdgcn_raw_buffer_load_b32(rc, lane * 128, 0, 0);
      asm volatile("" :: "v"(tc));
      lds_await<4>(L + L_QRDY, 1);
    }
    pt = (t0 + GM_TA) << 4;
    const GAS u16* xp = (const GAS u16*)(a.x + b * DM);
    float xv[9];
#pragma unroll
    for (int i = 0; i < 9; ++i) xv[i] = bf2f(xp[lane + 64 * i]);
    const int col0 = 64 * ((9 * h) / 4 + m), sl = 4 * (c >> 3) + g;
    float b1v[4];
#pragma unroll
    for (int r = 0; r < 4; ++r) b1v[r] = bf2f(((const GAS u16*)a.b1)[col0 + 8 * (4 * (g & 1) + r) + (c & 7)]);
    if (m == 0) {
      const GAS u16* gp = (const GAS u16*)a.gw;
      float gs = 0.f;
#pragma unroll
      for (int i = 0; i < 9; ++i) gs += rbf(xv[i] * bf2f(gp[lane + 64 * i]));
      const float logit = rbf(wave_sum(gs));
      const float gate = rbf(1.f / (1.f + __expf(-logit)));
      if (lane == 0) *(LAS float*)(L + L_GATE) = gate;
      if (lane < 16 || (lane < 32 && h < 4)) ((LAS float*)(L + L_BT))[lane] = rbf(1.f - gate) * bf2f(((const GAS u16*)a.b2)[16 * (h + 32 * g) + c]);
    }
    float sum = 0.f;
#pragma unroll
    for (int i = 0; i < 9; ++i) sum += xv[i];
    const float mean = wave_sum(sum) * (1.f / DM);
    float sq = 0.f;
#pragma unroll
    for (int i = 0; i < 9; ++i) sq += (xv[i] - mean) * (xv[i] - mean);
    const float var = rbf(wave_sum(sq) * (1.f / DM));
    const float mb = rbf(mean), sd = rbf(sqrtf(rbf(var + 1e-5f)));
    LAS u16* yl = (LAS u16*)(L + L_Y + m * 1152);
#pragma unroll
    for (int i = 0; i < 9; ++i) yl[72 * (lane & 7) + (lane >> 3) + 8 * i] = f2bf(rbf(xv[i] - mb) / sd);
    LDS_WAIT();
    unsigned mask[4];
#pragma unroll
    for (int r = 0; r < 4; ++r) mask[r] = ((c & 7) >> 1) == r ? ((c & 1) ? 0xffff0000u : 0x0000ffffu) : 0u;
    const int lo = col0 > FS * h ? col0 : FS * h, hi = col0 + 64 < FS * h + FS ? col0 + 64 : FS * h + FS;
    const int nrg = (hi - lo) >> 3;
    const rsrc_t r1s = mk_rsrc(a.w1 + col0, (DM - 1) * FF * 2 + 128);
    const rsrc_t r2s = mk_rsrc(a.w2 + (size_t)lo * DM, nrg * 8 * DM * 2);
    const int v1 = sl * (FF * 2) + (c & 7) * 16, v2 = sl * (DM * 2) + (c & 7) * 16;
    v4u rg3[3];
#pragma unroll
    for (int i = 0; i < 3; ++i) { PACE(GM_AD * 16 / 144); rg3[i] = __builtin_amdgcn_raw_buffer_load_b128(r1s, v1, i * (8 * FF * 2), 0); SCHED_FENCE(); }
    f32x4 h1 = {0.f, 0.f, 0.f, 0.f};
#pragma unroll 1
    for (int o = 0; o < 72; o += 6) {
      const bool last = o == 66;
      const rsrc_t rn = mk_rsrc(last ? a.w2 + (size_t)lo * DM : a.w1 + col0, last ? nrg * 8 * DM * 2 : (DM - 1) * FF * 2 + 128);
      const int vn = last ? v2 : v1;
      unsigned yy[3];
#pragma unroll
      for (int i = 0; i < 3; ++i) yy[i] = *(const LAS unsigned*)(yl + 72 * sl + o + 2 * i);
#pragma unroll
      for (int i = 0; i < 6; ++i) {
        const unsigned dup = __builtin_amdgcn_perm(yy[i >> 1], yy[i >> 1], (i & 1) ? 0x03020302u : 0x01000100u);
        h1 = mfma(rg3[i % 3], v4u{dup & mask[0], dup & mask[1], dup & mask[2], dup & mask[3]}, h1);
        asm volatile("" : "+v"(h1));
        SCHED_FENCE();
        PACE(GM_AD * 16 / 144);
        if (i < 3) rg3[i] = __builtin_amdgcn_raw_buffer_load_b128(r1s, v1, (o + i + 3) * (8 * FF * 2), 0);
        else rg3[i - 3] = __builtin_amdgcn_raw_buffer_load_b128(rn, vn, last ? (i - 3) * 128 : (o + i + 3) * (8 * FF * 2), 0);
        SCHED_FENCE();
      }
    }
    LAS u16* hid = (LAS u16*)(L + L_HID + m * 128);
#pragma unroll
    for (int r = 0; r < 4; ++r) {
      const float pre = rbf(rbf(fold_halves(h1[r])) + b1v[r]);
      const float inner = 0.7978845608028654f * (pre + 0.044715f * pre * pre * pre);
      const float th = 1.f - 2.f / (1.f + __expf(2.f * inner));
      if (lane < 32 && c < 8) hid[8 * (4 * g + r) + c] = f2bf(0.5f * pre * (1.f + th));
    }
    LDS_WAIT();
    const unsigned step2 = (GM_AD * 16 / 18) / nrg;
    f32x4 o2[9];
#pragma unroll
    for (int j = 0; j < 9; ++j) o2[j] = f32x4{0.f, 0.f, 0.f, 0.f};
#pragma unroll 1
    for (int rg = 0; rg < nrg; ++rg) {
      const unsigned u = hid[lo - col0 + 8 * rg + sl];
      const unsigned dup = u | (u << 16);
      const v4u bf = {dup & mask[0], dup & mask[1], dup & mask[2], dup & mask[3]};
#pragma unroll
      for (int j = 0; j < 9; ++j) {
        o2[j] = mfma(rg3[j % 3], bf, o2[j]);
        asm volatile("" : "+v"(o2[j]));
        SCHED_FENCE();
        if (j < 6 || rg + 1 < nrg) PACE(step2);
        rg3[j % 3] = __builtin_amdgcn_raw_buffer_load_b128(r2s, v2 + ((j + 3) % 9) * 128, (rg + (j >= 6)) * (8 * DM * 2), 0);
        SCHED_FENCE();
      }
    }
    LAS float* mp = (LAS float*)(L + L_MLP + m * 2304);
#pragma unroll
    for (int j = 0; j < 9; ++j) {
      f32x4 t;
#pragma unroll
      for (int r = 0; r < 4; ++r) t[r] = fold_halves(o2[j][r]);
      if (lane < 32 && c < 8) {
#pragma unroll
        for (int r = 0; r < 4; ++r) mp[64 * j + 8 * (4 * g + r) + c] = t[r];
      }
    }
  }
  lds_signal(L + L_DONE, lane);
  lds_await<8>(L + L_DONE, NWA);
  v4u bf[9][4];
  {
    LAS unsigned char* tb = L + L_WO + (4 * g + q4) * 1152 + ((w ^ ((2 * g + (q4 >> 1)) & 3)) * 32) + 8 * p4;
#pragma unroll
    for (int j = 0; j < 9; ++j)
#pragma unroll
      for (int ks = 0; ks < 4; ++ks) bf[j][ks] = tr_frag(tb + ks * 32 * 1152 + j * 128, tb + (ks * 32 + 16) * 1152 + j * 128);
  }
  const float gate = *(const LAS float*)(L + L_GATE);
  const float omg = rbf(1.f - gate);
  v2u tag = tag_of(*(const LAS u64*)(L + L_EPOCH));
  const int jg = 2 * (g & 1) + (g >> 1);
  float base[3];
#pragma unroll
  for (int i = 0; i < 3; ++i) {
    const int n = 16 * (w + 4 * (i < 2 ? 4 * i + jg : 8)) + c;
    const LAS float* mp = (const LAS float*)(L + L_MLP);
    base[i] = omg * ((mp[n] + mp[DM + n]) + mp[2 * DM + n]);
  }
  int ox = (((w + 4 * jg) * 32 + h) * 16 + c) * 8;
  int ox2 = g == 0 ? ox + 2 * 16 * 4096 : 36 * 4096;
  f32x4 cw;
  {
#pragma unroll
    for (int i = 0; i < NWS; ++i) lds_await<8>(L + L_ML + 8 * i + 4, 1);
    const LAS float* ml = (const LAS float*)(L + L_ML);
    const LAS float* lpp = (const LAS float*)(L + L_LP);
    const float mx = fmaxf(fmaxf(ml[0], ml[2]), fmaxf(ml[4], ml[6]));
#pragma unroll
    for (int i = 0; i < NWS; ++i) cw[i] = __builtin_amdgcn_exp2f((ml[2 * i] - mx) * LOG2E);
    const float den = wave_sum((cw[0] * lpp[lane] + cw[1] * lpp[64 + lane]) + (cw[2] * lpp[128 + lane] + cw[3] * lpp[192 + lane]));
    cw *= gate / den;
  }
  LAS unsigned char* ap0 = L + L_A + (c < 8 ? c : 8) * A_STR + 16 * g;
  LAS unsigned char* ap1 = L + L_A + (c < 8 ? 8 : c - 8) * A_STR + 16 * g;
  unsigned char* oxb = a.ws + W_OX + (size_t)b * 36 * 4096;
#pragma unroll
  for (int j = 0; j < 9; ++j) asm volatile("" : "+v"(bf[j][0]), "+v"(bf[j][1]), "+v"(bf[j][2]), "+v"(bf[j][3]));
  asm volatile("" : "+v"(base[0]), "+v"(base[1]), "+v"(base[2]), "+v"(ox), "+v"(ox2), "+v"(cw), "+v"(ap0), "+v"(ap1), "+v"(tag), "+s"(oxb));
  const rsrc_t ro = mk_rsrc(oxb, 36 * 4096);
  WG_BARRIER();
  v4u af[2][4];
#pragma unroll
  for (int v = 0; v < 2; ++v)
#pragma unroll
    for (int ks = 0; ks < 4; ++ks) af[v][ks] = *(const LAS v4u*)((v ? ap1 : ap0) + 64 * ks);
  SCHED_FENCE();
  f32x4 oc[5];
#pragma unroll
  for (int p = 0; p < 5; ++p) oc[p] = f32x4{0.f, 0.f, 0.f, 0.f};
  auto put = [&](int i, f32x4 u, f32x4 v) {
    const float tu = fmaf(cw[3], u[3], fmaf(cw[2], u[2], fmaf(cw[1], u[1], cw[0] * u[0])));
    const float tv = fmaf(cw[3], v[3], fmaf(cw[2], v[2], fmaf(cw[1], v[1], cw[0] * v[0])));
    auto sw = __builtin_amdgcn_permlane16_swap(__float_as_uint(tu), __float_as_uint(tv), false, false);
    const v2u gr = granule(tag, (__uint_as_float(sw[0]) + __uint_as_float(sw[1])) + base[i]);
    __builtin_amdgcn_raw_buffer_store_b64(gr, ro, i < 2 ? ox : ox2, i == 1 ? 16 * 4096 : 0, AUX_SC1);
  };
#pragma unroll
  for (int ks = 0; ks < 4; ++ks) {
    oc[0] = mfma(af[0][ks], bf[0][ks], oc[0]); oc[1] = mfma(af[0][ks], bf[2][ks], oc[1]); oc[4] = mfma(af[0][ks], bf[8][ks], oc[4]);
    SCHED_FENCE();
  }
#pragma unroll
  for (int ks = 0; ks < 4; ++ks) { oc[0] = mfma(af[1][ks], bf[1][ks], oc[0]); oc[1] = mfma(af[1][ks], bf[3][ks], oc[1]); }
  put(2, oc[4], oc[4]);
  SCHED_FENCE();
  oc[2] = mfma(af[0][0], bf[4][0], oc[2]); oc[3] = mfma(af[0][0], bf[6][0], oc[3]);
  SCHED_FENCE();
#pragma unroll
  for (int s8 = 1; s8 < 8; ++s8) {
    oc[2] = mfma(af[s8 >> 2][s8 & 3], bf[4 + (s8 >> 2)][s8 & 3], oc[2]);
    oc[3] = mfma(af[s8 >> 2][s8 & 3], bf[6 + (s8 >> 2)][s8 & 3], oc[3]);
  }
  put(0, oc[0], oc[1]);
#pragma unroll
  for (int i = 0; i < 14; ++i) { __builtin_amdgcn_sched_group_barrier(0x8, 1, 0); __builtin_amdgcn_sched_group_barrier(0x2, 2, 0); }
  SCHED_FENCE();
  put(1, oc[2], oc[3]);
}

}

extern "C" void kernel_launch(void* const* d_in, const int* in_sizes, int n_in, void* d_out, int out_size, void* d_ws,
                              size_t ws_size, hipStream_t stream) {
  (void)in_sizes; (void)n_in; (void)out_size; (void)ws_size;
  gm::Args a;
  a.x = (const gm::u16*)d_in[0]; a.k = (const gm::u16*)d_in[1]; a.v = (const gm::u16*)d_in[2];
  a.wq = (const gm::u16*)d_in[3]; a.wo = (const gm::u16*)d_in[4];
  a.w1 = (const gm::u16*)d_in[6]; a.b1 = (const gm::u16*)d_in[7]; a.w2 = (const gm::u16*)d_in[8]; a.b2 = (const gm::u16*)d_in[9];
  a.gw = (const gm::u16*)d_in[10];
  a.out = (gm::u16*)d_out; a.ws = (unsigned char*)d_ws;
  (void)hipFuncSetAttribute((const void*)gm::fused_kernel, hipFuncAttributeMaxDynamicSharedMemorySize, gm::L_BYTES);
  hipLaunchKernelGGL(gm::fused_kernel, dim3(8, 4, 8), dim3(gm::NTHR), gm::L_BYTES, stream, a);
}
```
